```python
import torch

def _softmax(x, axis=-1):
    return torch.softmax(x, dim=axis)
def _gelu(x):
    return torch.nn.functional.gelu(x, approximate="tanh")
def _silu(x):
    return torch.nn.functional.silu(x)
def _sigmoid(x):
    return torch.sigmoid(x)
def _layernorm(x, axis=-1, eps=1e-5):
    m = x.mean(dim=axis, keepdim=True)
    v = x.var(dim=axis, keepdim=True, unbiased=False)
    return (x - m) / torch.sqrt(v + eps)
def _scatter_add(tgt, idx, src, axis=0):
    return tgt.clone().index_add_(axis, idx, src)
def _segment_sum(src, seg, n):
    out = torch.zeros((n,) + src.shape[1:], dtype=src.dtype)
    return out.index_add_(0, seg, src)
def _pad(x, pw):
    flat = tuple(v for pair in reversed(pw) for v in pair)
    return torch.nn.functional.pad(x, flat)

kernel_name = 'interleave_layers__ssm_conv_s4d__linear_attn_block__prompt'

def setup_inputs():

    _w = (lambda a: (lambda t: t.to(torch.bfloat16) if t.is_floating_point() else t)(torch.as_tensor(a)))
    def _A_setup():
        Bsz, L, D, N = 1, 2048, 512, 128
        return {
            "u": _w((lambda *s: torch.randn(*s, dtype=torch.bfloat16) * 0.05)(Bsz, L, D)),
            "log_A_real": _w((lambda *s: torch.randn(*s, dtype=torch.bfloat16) * 1.6)(D, N) * 0.5),
            "A_imag": _w((lambda *s: torch.randn(*s, dtype=torch.bfloat16) * 0.05)(D, N) * 3.14),
            "C_real": _w((lambda *s: torch.randn(*s, dtype=torch.bfloat16) * 0.05)(D, N) / N ** 0.5),
            "C_imag": _w((lambda *s: torch.randn(*s, dtype=torch.bfloat16) * 0.05)(D, N) / N ** 0.5),
            "log_dt": _w((lambda *s: torch.randn(*s, dtype=torch.bfloat16) * 0.05)(D) * 0.5 - 4.0),
            "D_skip": _w((lambda *s: torch.randn(*s, dtype=torch.bfloat16) * 0.05)(D)),
        }
    _A_in = _A_setup()

    def _B_setup():
        B, S, D = 1, 2048, 512
        return {
            "x": _w((lambda *s: torch.randn(*s, dtype=torch.bfloat16) * 0.05)(B, S, D)),
            "wqkv": _w((lambda *s: torch.randn(*s, dtype=torch.bfloat16) * 0.05)(D, 3 * D)),
            "wo": _w((lambda *s: torch.randn(*s, dtype=torch.bfloat16) * 0.05)(D, D)),
        }
    _B_in = _B_setup()

    return {
        "A_u": _A_in["u"],
        "A_log_A_real": _A_in["log_A_real"],
        "A_A_imag": _A_in["A_imag"],
        "A_C_real": _A_in["C_real"],
        "A_C_imag": _A_in["C_imag"],
        "A_log_dt": _A_in["log_dt"],
        "A_D_skip": _A_in["D_skip"],
        "B_x": _B_in["x"],
        "B_wqkv": _B_in["wqkv"],
        "B_wo": _B_in["wo"],
    }

def reference(A_u, A_log_A_real, A_A_imag, A_C_real, A_C_imag, A_log_dt, A_D_skip, B_x, B_wqkv, B_wo):
    def _A_ref(u, log_A_real, A_imag, C_real, C_imag, log_dt, D_skip):
        L = u.shape[1]
        dt = torch.exp(log_dt)
        A = -torch.exp(log_A_real) + 1j * A_imag
        C = (C_real + 1j * C_imag) * dt[:, None]
        pos = (lambda x, dt: x.to(dt))(torch.arange(L), log_dt.dtype)
        P = torch.exp((A * dt[:, None])[:, :, None] * pos[None, None, :])
        K = 2.0 * torch.einsum("dn,dnl->dl", C, P).real
        L2 = 2 * L
        u_t = (lambda x, *d: x.permute(*d))(u, 0, 2, 1)
        Kf = (lambda x, *a, **k: torch.fft.rfft(x.float(), *a, **k))(K, L2)
        Uf = (lambda x, *a, **k: torch.fft.rfft(x.float(), *a, **k))(u_t, L2)
        y = (lambda x, *a, **k: torch.fft.irfft(x, *a, **k).to(torch.bfloat16))(Uf * Kf[None, :, :], L2)[:, :, :L]
        y = (lambda x, *d: x.permute(*d))(y, 0, 2, 1)
        return y + u * D_skip[None, None, :]

    def _B_ref(x, wqkv, wo):
        B, S, D = x.shape
        H = 32; Dh = D // H
        qkv = (lambda x, *d: x.permute(*d))((x @ wqkv).reshape(B, S, 3, H, Dh), 2, 0, 3, 1, 4)
        q, k, v = qkv[0], qkv[1], qkv[2]
        phi = lambda t: torch.where(t > 0, t + 1, torch.exp(t)) + 1e-6
        pq, pk = phi(q), phi(k)
        kv = torch.einsum("bhsd,bhse->bhde", pk, v)
        z = torch.einsum("bhsd->bhd", pk)
        num = torch.einsum("bhqd,bhde->bhqe", pq, kv)
        den = torch.einsum("bhqd,bhd->bhq", pq, z)[..., None] + 1e-6
        o = num / den
        return (lambda x, *d: x.permute(*d))(o, 0, 2, 1, 3).reshape(B, S, D) @ wo

    x = A_u
    x = x + _A_ref(_layernorm(x, axis=-1), A_log_A_real, A_A_imag, A_C_real, A_C_imag, A_log_dt, A_D_skip)
    x = x + _B_ref(_layernorm(x, axis=-1), B_wqkv, B_wo)
    x = x + _A_ref(_layernorm(x, axis=-1), A_log_A_real, A_A_imag, A_C_real, A_C_imag, A_log_dt, A_D_skip)
    x = x + _B_ref(_layernorm(x, axis=-1), B_wqkv, B_wo)
    return x
```

```cpp
#include <hip/hip_runtime.h>
#include <hip/hip_bf16.h>
#include <stdint.h>

typedef unsigned u32;
typedef unsigned u32x4 __attribute__((ext_vector_type(4)));
typedef unsigned u32x2 __attribute__((ext_vector_type(2)));
typedef float f32x4 __attribute__((ext_vector_type(4)));
typedef float f32x2 __attribute__((ext_vector_type(2)));
typedef short s16x4 __attribute__((ext_vector_type(4)));
typedef __bf16 bf16x8 __attribute__((ext_vector_type(8)));
typedef __bf16 bf16x2 __attribute__((ext_vector_type(2)));
#define LAS __attribute__((address_space(3)))
#define DEV __device__ __forceinline__

constexpr int L = 2048, D = 512, NB = 256, NT = 512;
constexpr u32 O_WQ = 0;
constexpr u32 O_WO = O_WQ + 1536 * 512 * 2;
constexpr u32 O_KR = O_WO + 512 * 512 * 2;
constexpr u32 KR_STR = 2064 * 2;
constexpr u32 XSZ = L * D * 2;
constexpr u32 O_X0 = O_KR + 512 * KR_STR;
constexpr u32 O_ST0 = O_X0 + XSZ;
constexpr u32 O_X1 = O_ST0 + L * 8;
constexpr u32 O_X2 = O_X1 + XSZ;
constexpr u32 O_X3 = O_X2 + XSZ;
constexpr u32 O_ST2 = O_X3 + XSZ;
constexpr u32 O_PQ = O_ST2 + 4 * L * 8;
constexpr u32 O_KVP = O_PQ + 2 * XSZ;
constexpr u32 KVP_SZ = 32 * 32 * 1024;
constexpr u32 O_ZP = O_KVP + 2 * KVP_SZ;
constexpr u32 ZP_SZ = 32 * 32 * 64;
constexpr u32 O_KVT = O_ZP + 2 * ZP_SZ;
constexpr u32 KVT_SZ = 32 * 512;
constexpr u32 O_ZT = O_KVT + 2 * KVT_SZ;
constexpr u32 ZT_SZ = 32 * 32;
constexpr u32 O_END = O_ZT + 2 * ZT_SZ;

struct Params {
  const u32* u; const unsigned short* logA; const unsigned short* Aim; const unsigned short* Cre; const unsigned short* Cim;
  const unsigned short* logdt; const unsigned short* Dskip; const unsigned short* wqkv; const unsigned short* wo;
  char* ws; unsigned short* out;
};

DEV float bf2f(u32 h) { return __builtin_bit_cast(float, h << 16); }
DEV float bflo(u32 w) { return __builtin_bit_cast(float, w << 16); }
DEV float bfhi(u32 w) { return __builtin_bit_cast(float, w & 0xffff0000u); }
DEV u32 pk2(float lo, float hi) { f32x2 f = {lo, hi}; return __builtin_bit_cast(u32, __builtin_convertvector(f, bf16x2)); }
DEV float rbf(float x) { return bflo(pk2(x, x)); }
DEV f32x4 mfma32(u32x4 a, u32x4 b, f32x4 c) {
  return __builtin_amdgcn_mfma_f32_16x16x32_bf16(__builtin_bit_cast(bf16x8, a), __builtin_bit_cast(bf16x8, b), c, 0, 0, 0);
}
DEV f32x4 mfma16(u32x2 a, u32x2 b, f32x4 c) {
  return __builtin_amdgcn_mfma_f32_16x16x16bf16_1k(__builtin_bit_cast(s16x4, a), __builtin_bit_cast(s16x4, b), c, 0, 0, 0);
}
DEV float dot2(u32 a, u32 b, float c) { return __builtin_amdgcn_fdot2_f32_bf16(__builtin_bit_cast(bf16x2, a), __builtin_bit_cast(bf16x2, b), c, false); }
typedef __amdgpu_buffer_rsrc_t rsrc_t;
DEV rsrc_t mk_rsrc(const void* p, u32 bytes) { return __builtin_amdgcn_make_buffer_rsrc((void*)p, (short)0, (int)bytes, 0x00020000); }
template <int AUX> DEV u32x4 ld16(rsrc_t r, u32 off) { return __builtin_amdgcn_raw_buffer_load_b128(r, off, 0, AUX); }
template <int AUX> DEV u32x2 ld8(rsrc_t r, u32 off) { return __builtin_amdgcn_raw_buffer_load_b64(r, off, 0, AUX); }
template <int AUX> DEV u32 ld4(rsrc_t r, u32 off) { return __builtin_amdgcn_raw_buffer_load_b32(r, off, 0, AUX); }
template <int AUX> DEV void st16(rsrc_t r, u32 off, u32x4 v) { __builtin_amdgcn_raw_buffer_store_b128(v, r, off, 0, AUX); }
template <int AUX> DEV void st8(rsrc_t r, u32 off, u32x2 v) { __builtin_amdgcn_raw_buffer_store_b64(v, r, off, 0, AUX); }
template <int AUX> DEV void st4(rsrc_t r, u32 off, u32 v) { __builtin_amdgcn_raw_buffer_store_b32(v, r, off, 0, AUX); }
DEV u32x4 lds_ld16(const char* s, u32 off) { return *(const LAS u32x4*)((const LAS char*)s + off); }
DEV u32x2 lds_ld8(const char* s, u32 off) { return *(const LAS u32x2*)((const LAS char*)s + off); }
DEV u32 lds_ld4(const char* s, u32 off) { return *(const LAS u32*)((const LAS char*)s + off); }
DEV void lds_st16(char* s, u32 off, u32x4 v) { *(LAS u32x4*)((LAS char*)s + off) = v; }
DEV void lds_st8(char* s, u32 off, u32x2 v) { *(LAS u32x2*)((LAS char*)s + off) = v; }
DEV void lds_st4(char* s, u32 off, u32 v) { *(LAS u32*)((LAS char*)s + off) = v; }
DEV u32x2 lds_tr(const char* s, u32 off) {
  return __builtin_bit_cast(u32x2, __builtin_amdgcn_ds_read_tr16_b64_v4i16((LAS s16x4*)((LAS char*)s + off)));
}
DEV float f_(u32 x) { return __builtin_bit_cast(float, x); }
DEV u32 u_(float x) { return __builtin_bit_cast(u32, x); }
DEV float xor16(float v) { return __shfl_xor(v, 16); }
DEV float xor32(float v) { return __shfl_xor(v, 32); }
DEV int permk(int g, int j) { return j < 4 ? 4 * g + j : 16 + 4 * g + (j - 4); }
DEV void ln_stats(float sum, float sq, float& m, float& rinv) {
  float mean = sum * (1.0f / 512.0f);
  float var = fmaxf(sq * (1.0f / 512.0f) - mean * mean, 0.0f);
  m = rbf(mean);
  float den = rbf(__builtin_sqrtf(rbf(rbf(var) + 1e-5f)));
  rinv = 1.0f / den;
}
DEV float phi(float t) { float p = t > 0.0f ? t + 1.0f : __expf(t); return rbf(rbf(p) + 1e-6f); }
constexpr u32 KA_ROW = 528;
constexpr u32 KA_TAB = 16 * KA_ROW;
constexpr u32 KA_G = 0, KA_E = 8 * KA_TAB;
constexpr u32 KA_KC = 16 * KA_TAB;
constexpr u32 KA_XT = KA_KC + 2 * 656 * 4;
constexpr u32 KA_LDS = KA_XT + 8 * 1040;
struct cf { float r, i; };
DEV cf cmul(cf a, cf b) { cf c; c.r = a.r * b.r - a.i * b.i; c.i = a.r * b.i + a.i * b.r; return c; }
DEV cf cexp_pow(float are, float aim, int k) {
  float s = (float)(1 << k);
  float mag = __expf(are * s);
  double rev = (double)aim * (0.15915494309189535 * (double)(1 << k));
  float fr = (float)(rev - __builtin_floor(rev));
  cf c; c.r = mag * __builtin_amdgcn_cosf(fr); c.i = mag * __builtin_amdgcn_sinf(fr); return c;
}
DEV void phase_A(const Params& p, char* sm) {
  const int tid = threadIdx.x, b = blockIdx.x, lane = tid & 63, w = tid >> 6;
  rsrc_t rw = mk_rsrc(p.ws, O_END);
  const int rg = (b & 7) * 32 + (b >> 3);
  const int row = rg * 8 + w;
  u32x4 xv = *(const u32x4*)(p.u + (size_t)row * 256 + lane * 4);
  u32 wv[8]; u32 wdst;
  {
    int g = b * NT + tid, ln = g & 63, lq = ln >> 4;
    const unsigned short* src; int ld, ks;
    if (g < 98304) {
      int f = g >> 6, ty = f % 3, f2 = f / 3, ks8 = f2 & 7, kh = (f2 >> 3) & 1, hh = (f2 >> 4) & 3, cb = f2 >> 6;
      ks = kh * 8 + ks8; src = p.wqkv + ty * 512 + (4 * cb + hh) * 16 + (ln & 15); ld = 1536; wdst = O_WQ + (u32)g * 16;
    } else {
      int g2 = g - 98304, f = g2 >> 6; ks = f & 15; int ww = (f >> 4) & 7, cb4 = f >> 7;
      src = p.wo + 128 * cb4 + 16 * ww + (ln & 15); ld = 512; wdst = O_WO + (u32)g2 * 16;
    }
#pragma unroll
    for (int j = 0; j < 8; ++j) wv[j] = src[(size_t)(32 * ks + permk(lq, j)) * ld];
  }
  const int n = tid & 127, ch = (tid >> 7) & 1, sih = tid >> 8;
  const int d = 2 * b + ch;
  float lA = bf2f(p.logA[d * 128 + n]), Ai = bf2f(p.Aim[d * 128 + n]);
  float Cr = bf2f(p.Cre[d * 128 + n]), Ci = bf2f(p.Cim[d * 128 + n]);
  float dt = rbf(expf(bf2f(p.logdt[d])));
  {
    float s = 0.f, q = 0.f;
#pragma unroll
    for (int j = 0; j < 4; ++j) { s = dot2(xv[j], 0x3f803f80u, s); q = dot2(xv[j], xv[j], q); }
#pragma unroll
    for (int o = 1; o < 64; o <<= 1) { s += __shfl_xor(s, o); q += __shfl_xor(q, o); }
    if (lane == 0) { u32x2 v = {u_(s), u_(q)}; st8<0>(rw, O_ST0 + row * 8, v); }
    lds_st16(sm, KA_XT + w * 1040 + lane * 16, xv);
  }
  { u32x4 v = {wv[0] | (wv[1] << 16), wv[2] | (wv[3] << 16), wv[4] | (wv[5] << 16), wv[6] | (wv[7] << 16)}; st16<0>(rw, wdst, v); }
  float are = -rbf(expf(lA)) * dt, aim = Ai * dt;
  cf C; C.r = Cr * dt; C.i = Ci * dt;
#pragma unroll
  for (int t = 0; t < 2; ++t) {
    const int si = sih + 2 * t;
    const u32 tb = (u32)(ch * 4 + si) * KA_TAB + n * 4;
    cf v0 = cexp_pow(are, aim, si), v1 = cmul(v0, v0), v2 = cmul(v1, v1), v3 = cmul(v2, v2);
    cf e[8]; e[0].r = 1.f; e[0].i = 0.f; e[1] = v0; e[2] = v1; e[3] = cmul(v1, v0); e[4] = v2;
    e[5] = cmul(v2, v0); e[6] = cmul(v2, v1); e[7] = cmul(v2, e[3]);
#pragma unroll
    for (int j = 0; j < 8; ++j) {
      lds_st4(sm, KA_E + tb + j * KA_ROW, pk2(e[j].r, e[j].i));
      cf f = cmul(e[j], v3);
      lds_st4(sm, KA_E + tb + (j + 8) * KA_ROW, pk2(f.r, f.i));
    }
    cf g0 = cexp_pow(are, aim, 4 + si), g1 = cmul(g0, g0), g2 = cmul(g1, g1), g3 = cmul(g2, g2);
    cf h[8]; h[0] = (si == 0) ? C : cmul(C, g3);
    h[1] = cmul(h[0], g0); h[2] = cmul(h[0], g1); h[3] = cmul(h[1], g1);
#pragma unroll
    for (int j = 0; j < 4; ++j) h[4 + j] = cmul(h[j], g2);
#pragma unroll
    for (int j = 0; j < 8; ++j) {
      lds_st4(sm, KA_G + tb + j * KA_ROW, pk2(h[j].r, -h[j].i));
      cf f = cmul(h[j], g3);
      lds_st4(sm, KA_G + tb + (j + 8) * KA_ROW, pk2(f.r, -f.i));
    }
  }
  __syncthreads();
  {
    u32 v[8];
#pragma unroll
    for (int r = 0; r < 8; ++r) v[r] = *(const LAS unsigned short*)((LAS char*)sm + KA_XT + r * 1040 + tid * 2);
    u32x4 o = {v[0] | (v[1] << 16), v[2] | (v[3] << 16), v[4] | (v[5] << 16), v[6] | (v[7] << 16)};
    st16<0>(rw, O_X0 + (((rg >> 2) * 512 + tid) * 32 + (rg & 3) * 8) * 2, o);
  }
  {
    const int wc = w >> 2, si = w & 3, lq = lane >> 4, r16 = lane & 15;
    const u32 base = (u32)(wc * 4 + si) * KA_TAB + r16 * KA_ROW + lq * 16;
    f32x4 acc = {0.f, 0.f, 0.f, 0.f};
#pragma unroll
    for (int ks = 0; ks < 8; ++ks) acc = mfma32(lds_ld16(sm, KA_G + base + ks * 64), lds_ld16(sm, KA_E + base + ks * 64), acc);
#pragma unroll
    for (int r = 0; r < 4; ++r) {
      int i = 4 * lq + r;
      int idx = si == 0 ? 16 * i + r16 : 256 + (si - 1) * 128 + 16 * i + r16;
      bool ok = si == 0 || i < 8 || (si == 3 && i == 8 && r16 == 0);
      if (ok) lds_st4(sm, KA_KC + (wc * 656 + idx) * 4, u_(2.0f * acc[r]));
    }
  }
  __syncthreads();
  {
    const int c2 = tid >> 8, m = tid & 255;
    u32 o[4];
#pragma unroll
    for (int j = 0; j < 4; ++j) {
      float kv[2];
#pragma unroll
      for (int h = 0; h < 2; ++h) {
        int l = 2047 - (8 * m + 2 * j + h);
        int e = l < 256 ? 0 : (l < 512 ? 1 : (l < 1024 ? 2 : 3));
        int mm = (l + ((1 << e) >> 1) - 1 + ((l >> e) & 1)) >> e;
        int idx = e == 0 ? l : 256 + (e - 1) * 128 + (mm - 128);
        kv[h] = f_(lds_ld4(sm, KA_KC + (c2 * 656 + idx) * 4));
      }
      o[j] = pk2(kv[0], kv[1]);
    }
    u32x4 ov = {o[0], o[1], o[2], o[3]};
    const u32 kb = O_KR + (u32)(2 * b + c2) * KR_STR;
    st16<0>(rw, kb + m * 16, ov);
    if (m < 2) { u32x4 z = {0u, 0u, 0u, 0u}; st16<0>(rw, kb + 4096 + m * 16, z); }
  }
}
constexpr u32 C_G = 0, C_GSTR = 2064 * 8;
constexpr u32 C_U = C_G + 2 * C_GSTR;
constexpr u32 C_ST = C_U + 2 * 4608;
constexpr u32 C_RED = C_ST + L * 8;
constexpr u32 C_LDS = C_RED + 65536;
DEV void conv_build_G(rsrc_t rw, char* sm, int d0) {
  const int tid = threadIdx.x, ch = tid >> 8, m = tid & 255;
  const u32 kb = O_KR + (u32)(d0 + ch) * KR_STR + m * 16;
  u32x4 a = ld16<0>(rw, kb); u32x2 bb = ld8<0>(rw, kb + 16);
  u32 r[6] = {a[0], a[1], a[2], a[3], bb[0], bb[1]};
  const u32 gb = C_G + ch * C_GSTR + m * 64;
#pragma unroll
  for (int i = 0; i < 4; ++i) {
    u32x4 v = {r[i], r[i + 1], __builtin_amdgcn_alignbit(r[i + 1], r[i], 16), __builtin_amdgcn_alignbit(r[i + 2 > 5 ? 5 : i + 2], r[i + 1], 16)};
    lds_st16(sm, gb + i * 16, v);
  }
  if (m == 255) {
    u32x4 z = {0u, 0u, 0u, 0u};
#pragma unroll
    for (int i = 0; i < 8; ++i) lds_st16(sm, gb + 64 + i * 16, z);
  }
  if (tid < 64) { u32x4 z = {0u, 0u, 0u, 0u}; lds_st16(sm, C_U + (tid >> 5) * 4608 + ((tid >> 4) & 1) * 2304 + (tid & 15) * 16, z); }
}
template <int NP, int AUX>
DEV void phase_C(const Params& p, rsrc_t rw, char* sm, int d0, u32 o_in, u32 o_st, u32 o_out) {
  const int tid = threadIdx.x, lane = tid & 63, w = tid >> 6, ch = tid >> 8, m = tid & 255;
  const u32 xoff = (((m >> 2) * 512 + d0 + ch) * 32 + (m & 3) * 8) * 2;
  u32x4 xv = ld16<AUX>(rw, o_in + xoff);
  {
    u32x4 sv[NP][2];
#pragma unroll
    for (int q = 0; q < NP; ++q) { sv[q][0] = ld16<AUX>(rw, o_st + q * L * 8 + tid * 32); sv[q][1] = ld16<AUX>(rw, o_st + q * L * 8 + tid * 32 + 16); }
    float mr[8];
#pragma unroll
    for (int r = 0; r < 4; ++r) {
      float s = 0.f, q2 = 0.f;
#pragma unroll
      for (int q = 0; q < NP; ++q) { s += f_(sv[q][r >> 1][(r & 1) * 2]); q2 += f_(sv[q][r >> 1][(r & 1) * 2 + 1]); }
      ln_stats(s, q2, mr[2 * r], mr[2 * r + 1]);
    }
    u32x4 a = {u_(mr[0]), u_(mr[1]), u_(mr[2]), u_(mr[3])}, bq = {u_(mr[4]), u_(mr[5]), u_(mr[6]), u_(mr[7])};
    lds_st16(sm, C_ST + tid * 32, a); lds_st16(sm, C_ST + tid * 32 + 16, bq);
  }
  const float dsk = bf2f(p.Dskip[d0 + ch]);
  __syncthreads();
  u32x4 uv;
  {
#pragma unroll
    for (int j = 0; j < 4; ++j) {
      u32x4 s = lds_ld16(sm, C_ST + (8 * m + 2 * j) * 8);
      float u0 = rbf(bflo(xv[j]) - f_(s[0])) * f_(s[1]), u1 = rbf(bfhi(xv[j]) - f_(s[2])) * f_(s[3]);
      uv[j] = pk2(u0, u1);
    }
    lds_st16(sm, C_U + ch * 4608 + (m & 1) * 2304 + (16 + (m >> 1)) * 16, uv);
  }
  __syncthreads();
  {
    const int wc = w >> 2, w4 = w & 3, lq = lane >> 4, r16 = lane & 15;
    const int j0 = (lq & 1) * 16 + (lq >> 1) * 8;
    f32x4 acc[8];
#pragma unroll
    for (int c = 0; c < 8; ++c) acc[c] = f32x4{0.f, 0.f, 0.f, 0.f};
#pragma unroll
    for (int rr = 0; rr < 2; ++rr) {
      const int rho = 2 * w4 + rr;
      const u32 ub = C_U + wc * 4608 + (lq >> 1) * 2304 + (15 - 2 * rho + r16 + (lq & 1)) * 16;
      const u32 gb = C_G + wc * C_GSTR + (2031 - 32 * rho - r16 + j0) * 8;
      u32x4 B[8];
#pragma unroll
      for (int k = 0; k < 8; ++k) B[k] = lds_ld16(sm, ub + k * 256);
#pragma unroll
      for (int g = 0; g < 8; ++g) {
        u32x2 a0 = lds_ld8(sm, gb - g * 2048), a1 = lds_ld8(sm, gb - g * 2048 + 32);
        u32x4 A = {a0[0], a0[1], a1[0], a1[1]};
#pragma unroll
        for (int c = g; c < 8; ++c) acc[c] = mfma32(A, B[c - g], acc[c]);
      }
    }
#pragma unroll
    for (int c = 0; c < 8; ++c)
      lds_st16(sm, C_RED + ((wc * 4 + w4) * 8 + c) * 1024 + lane * 16, __builtin_bit_cast(u32x4, acc[c]));
  }
  __syncthreads();
  {
    const int c = m >> 5, a = (m & 31) >> 1, lq0 = (m & 1) * 2;
    f32x4 y0 = {0.f, 0.f, 0.f, 0.f}, y1 = y0;
#pragma unroll
    for (int q = 0; q < 4; ++q) {
      const u32 o = C_RED + ((ch * 4 + q) * 8 + c) * 1024 + (lq0 * 16 + a) * 16;
      y0 += __builtin_bit_cast(f32x4, lds_ld16(sm, o)); y1 += __builtin_bit_cast(f32x4, lds_ld16(sm, o + 256));
    }
    u32x4 ov;
#pragma unroll
    for (int j = 0; j < 4; ++j) {
      float ya = j < 2 ? y0[2 * j] : y1[2 * j - 4], yb = j < 2 ? y0[2 * j + 1] : y1[2 * j - 3];
      float ra = rbf(rbf(ya) + rbf(bflo(uv[j]) * dsk)), rb = rbf(rbf(yb) + rbf(bfhi(uv[j]) * dsk));
      ov[j] = pk2(bflo(xv[j]) + ra, bfhi(xv[j]) + rb);
    }
    st16<AUX>(rw, o_out + xoff, ov);
  }
}

constexpr u32 Q_A = 0;
constexpr u32 Q_EX = 65536;
constexpr u32 Q_SP = Q_EX + 49152;
constexpr u32 Q_CS = Q_SP + 1024;
constexpr u32 Q_LDS = Q_CS + 4 * 2 * 3 * 16 * 4;
DEV void q_load_w(rsrc_t rw, u32x4 (&W)[8][3], int cb) {
  const int tid = threadIdx.x, lane = tid & 63, w = tid >> 6;
  const u32 base = O_WQ + (u32)(cb * 8 + w) * 24576 + lane * 16;
#pragma unroll
  for (int ks = 0; ks < 8; ++ks)
#pragma unroll
    for (int ty = 0; ty < 3; ++ty) W[ks][ty] = ld16<0>(rw, base + (ks * 3 + ty) * 1024);
}
DEV void q_colsum(char* sm, const u32x4 (&W)[8][3]) {
  const int tid = threadIdx.x, lane = tid & 63, w = tid >> 6;
#pragma unroll
  for (int ty = 0; ty < 3; ++ty) {
    float s = 0.f;
#pragma unroll
    for (int ks = 0; ks < 8; ++ks)
#pragma unroll
      for (int j = 0; j < 4; ++j) s = dot2(W[ks][ty][j], 0x3f803f80u, s);
    s += xor16(s); s += xor32(s);
    if (lane < 16) lds_st4(sm, Q_CS + ((w * 3 + ty) * 16 + lane) * 4, u_(s));
  }
}
template <int AUX>
DEV void phase_Q(rsrc_t rw, char* sm, const u32x4 (&W)[8][3], int rb, int cb, u32 o_x, u32 o_pq, u32 o_kvp, u32 o_zp) {
  const int tid = threadIdx.x, lane = tid & 63, w = tid >> 6, hh = w >> 1, kh = w & 1, lq = lane >> 4, r16 = lane & 15;
  {
    u32x4 av[8];
#pragma unroll
    for (int it = 0; it < 8; ++it) av[it] = ld16<AUX>(rw, o_x + (u32)rb * 65536 + (tid + it * 512) * 16);
#pragma unroll
    for (int it = 0; it < 8; ++it) {
      const int i = tid + it * 512, rbl = i >> 11, chn = (i & 2047) >> 2, pc = i & 3;
      const int c = rbl * 2 + (pc >> 1);
      lds_st16(sm, Q_A + chn * 128 + ((c ^ ((chn >> 1) & 3)) * 32) + (pc & 1) * 16, av[it]);
    }
  }
  __syncthreads();
  f32x4 acc[4][3];
#pragma unroll
  for (int rt = 0; rt < 4; ++rt)
#pragma unroll
    for (int ty = 0; ty < 3; ++ty) acc[rt][ty] = f32x4{0.f, 0.f, 0.f, 0.f};
  float ssum = 0.f, ssq = 0.f;
  const int tq = r16 >> 2, tp = r16 & 3;
#pragma unroll
  for (int ks = 0; ks < 8; ++ks) {
    const int ch0 = 32 * (kh * 8 + ks) + 4 * lq + tq, ch1 = ch0 + 16;
#pragma unroll
    for (int rt = 0; rt < 4; ++rt) {
      u32x2 x0 = lds_tr(sm, Q_A + ch0 * 128 + ((rt ^ ((ch0 >> 1) & 3)) * 32) + tp * 8);
      u32x2 x1 = lds_tr(sm, Q_A + ch1 * 128 + ((rt ^ ((ch1 >> 1) & 3)) * 32) + tp * 8);
      u32x4 X = {x0[0], x0[1], x1[0], x1[1]};
      acc[rt][0] = mfma32(W[ks][0], X, acc[rt][0]);
      acc[rt][1] = mfma32(X, W[ks][1], acc[rt][1]);
      acc[rt][2] = mfma32(X, W[ks][2], acc[rt][2]);
      if (rt == hh) {
#pragma unroll
        for (int j = 0; j < 4; ++j) { ssum = dot2(X[j], 0x3f803f80u, ssum); ssq = dot2(X[j], X[j], ssq); }
      }
    }
  }
  ssum += xor16(ssum); ssum += xor32(ssum); ssq += xor16(ssq); ssq += xor32(ssq);
  if (lane < 16) { u32x2 v = {u_(ssum), u_(ssq)}; lds_st8(sm, Q_SP + (kh * 64 + hh * 16 + lane) * 8, v); }
  const u32 ex = Q_EX + hh * 12288 + lane * 16;
  if (kh == 0) {
#pragma unroll
    for (int rt = 0; rt < 4; ++rt) lds_st16(sm, ex + rt * 1024, __builtin_bit_cast(u32x4, acc[rt][0]));
  } else {
#pragma unroll
    for (int rt = 0; rt < 4; ++rt) {
      lds_st16(sm, ex + (4 + rt * 2) * 1024, __builtin_bit_cast(u32x4, acc[rt][1]));
      lds_st16(sm, ex + (5 + rt * 2) * 1024, __builtin_bit_cast(u32x4, acc[rt][2]));
    }
  }
  __syncthreads();
  const int h = cb * 4 + hh;
  if (kh == 1) {
    f32x4 cs;
#pragma unroll
    for (int r = 0; r < 4; ++r) cs[r] = f_(lds_ld4(sm, Q_CS + (((hh * 2) * 3 + 0) * 16 + 4 * lq + r) * 4)) + f_(lds_ld4(sm, Q_CS + (((hh * 2 + 1) * 3 + 0) * 16 + 4 * lq + r) * 4));
#pragma unroll
    for (int rp = 0; rp < 2; ++rp) {
      u32x4 ov;
#pragma unroll
      for (int t = 0; t < 2; ++t) {
        const int rt = rp * 2 + t;
        f32x4 a = acc[rt][0] + __builtin_bit_cast(f32x4, lds_ld16(sm, ex + rt * 1024));
        u32x2 p0 = lds_ld8(sm, Q_SP + (rt * 16 + r16) * 8), p1 = lds_ld8(sm, Q_SP + (64 + rt * 16 + r16) * 8);
        float mean, rinv; ln_stats(f_(p0[0]) + f_(p1[0]), f_(p0[1]) + f_(p1[1]), mean, rinv);
        float qv[4];
#pragma unroll
        for (int r = 0; r < 4; ++r) qv[r] = phi(rbf((a[r] - mean * cs[r]) * rinv));
        ov[2 * t] = pk2(qv[0], qv[1]); ov[2 * t + 1] = pk2(qv[2], qv[3]);
      }
      st16<AUX>(rw, o_pq + (((rb * 2 + rp) * 32 + h) * 64 + lane) * 16, ov);
    }
  } else {
    const float csk = f_(lds_ld4(sm, Q_CS + (((hh * 2) * 3 + 1) * 16 + r16) * 4)) + f_(lds_ld4(sm, Q_CS + (((hh * 2 + 1) * 3 + 1) * 16 + r16) * 4));
    const float csv = f_(lds_ld4(sm, Q_CS + (((hh * 2) * 3 + 2) * 16 + r16) * 4)) + f_(lds_ld4(sm, Q_CS + (((hh * 2 + 1) * 3 + 2) * 16 + r16) * 4));
    f32x4 kv = {0.f, 0.f, 0.f, 0.f}, zz = kv;
#pragma unroll
    for (int rp = 0; rp < 2; ++rp) {
      u32x4 pk, vv;
#pragma unroll
      for (int t = 0; t < 2; ++t) {
        const int rt = rp * 2 + t;
        f32x4 ak = acc[rt][1] + __builtin_bit_cast(f32x4, lds_ld16(sm, ex + (4 + rt * 2) * 1024));
        f32x4 av = acc[rt][2] + __builtin_bit_cast(f32x4, lds_ld16(sm, ex + (5 + rt * 2) * 1024));
        float kf[4], vf[4];
#pragma unroll
        for (int r2 = 0; r2 < 2; ++r2) {
          u32x4 p0 = lds_ld16(sm, Q_SP + (rt * 16 + 4 * lq + 2 * r2) * 8), p1 = lds_ld16(sm, Q_SP + (64 + rt * 16 + 4 * lq + 2 * r2) * 8);
#pragma unroll
          for (int e = 0; e < 2; ++e) {
            float mean, rinv; ln_stats(f_(p0[2 * e]) + f_(p1[2 * e]), f_(p0[2 * e + 1]) + f_(p1[2 * e + 1]), mean, rinv);
            const int r = 2 * r2 + e;
            kf[r] = phi(rbf((ak[r] - mean * csk) * rinv));
            vf[r] = (av[r] - mean * csv) * rinv;
          }
        }
        pk[2 * t] = pk2(kf[0], kf[1]); pk[2 * t + 1] = pk2(kf[2], kf[3]);
        vv[2 * t] = pk2(vf[0], vf[1]); vv[2 * t + 1] = pk2(vf[2], vf[3]);
      }
      kv = mfma32(pk, vv, kv);
      zz = mfma32(pk, u32x4{0x3f803f80u, 0x3f803f80u, 0x3f803f80u, 0x3f803f80u}, zz);
    }
    st16<AUX>(rw, o_kvp + ((rb * 32 + h) * 64 + lane) * 16, __builtin_bit_cast(u32x4, kv));
    if (r16 == 0) st16<AUX>(rw, o_zp + (rb * 32 + h) * 64 + lq * 16, __builtin_bit_cast(u32x4, zz));
  }
}

constexpr u32 R_LDS = 32 * 40 * 4;
template <int AUX>
DEV void phase_R(rsrc_t rw, char* sm, int b, u32 o_kvp, u32 o_zp, u32 o_kvt, u32 o_zt) {
  const int tid = threadIdx.x, h = b >> 3, j = b & 7, rb = tid >> 4, q = tid & 15;
  if (q < 8) { u32x4 v = ld16<AUX>(rw, o_kvp + ((rb * 32 + h) * 64 + 8 * j + q) * 16); lds_st16(sm, (rb * 40 + q * 4) * 4, v); }
  else if (q == 8) { u32x2 v = ld8<AUX>(rw, o_zp + (rb * 32 + h) * 64 + j * 8); lds_st8(sm, (rb * 40 + 32) * 4, v); }
  __syncthreads();
  if (tid < 64) {
    const int e = tid < 34 ? tid : 0;
    float s = 0.f;
#pragma unroll
    for (int r = 0; r < 32; ++r) s += f_(lds_ld4(sm, (r * 40 + e) * 4));
    float o = __shfl_down(s, 1);
    float o2 = __shfl_down(s, 2), o3 = __shfl_down(s, 3);
    if (tid < 32 && (tid & 3) == 0) { u32x2 v = {pk2(s, o), pk2(o2, o3)}; st8<AUX>(rw, o_kvt + (h * 64 + 8 * j + (tid >> 2)) * 8, v); }
    if (tid == 32) st4<AUX>(rw, o_zt + h * 32 + j * 4, pk2(s, o));
  }
}

constexpr u32 O_OF = 0;
constexpr u32 O_T = 32768;
constexpr u32 O_PS = O_T + 128 * 144;
constexpr u32 O_LDS = O_PS + 8 * 32 * 8;
DEV void o_load_w(rsrc_t rw, u32x4 (&Wo)[16], int cb4) {
  const int tid = threadIdx.x, lane = tid & 63, w = tid >> 6;
#pragma unroll
  for (int ks = 0; ks < 16; ++ks) Wo[ks] = ld16<0>(rw, O_WO + ((cb4 * 8 + w) * 16 + ks) * 1024 + lane * 16);
}
template <int AUX, bool FINAL>
DEV void phase_O(const Params& p, rsrc_t rw, char* sm, const u32x4 (&Wo)[16], int rb, int cb4, u32 o_pq, u32 o_kvt, u32 o_zt,
                 u32 o_xin, u32 o_xout, u32 o_st) {
  const int tid = threadIdx.x, lane = tid & 63, w = tid >> 6, lq = lane >> 4, r16 = lane & 15;
  const int ecol = tid >> 2, epc = tid & 3;
  const u32 xoff = ((rb * 512 + cb4 * 128 + ecol) * 32 + epc * 8) * 2;
  u32x4 xin = ld16<AUX>(rw, o_xin + xoff);
  {
    u32x4 pq[4]; u32x2 kv[4], zt[4];
#pragma unroll
    for (int i = 0; i < 4; ++i) {
      const int h = 4 * w + i;
      pq[i] = ld16<AUX>(rw, o_pq + ((rb * 32 + h) * 64 + lane) * 16);
      kv[i] = ld8<AUX>(rw, o_kvt + (h * 64 + lane) * 8);
      zt[i] = ld8<AUX>(rw, o_zt + h * 32 + lq * 8);
    }
#pragma unroll
    for (int hp = 0; hp < 2; ++hp)
#pragma unroll
      for (int rt = 0; rt < 2; ++rt) {
        u32x4 of;
#pragma unroll
        for (int t = 0; t < 2; ++t) {
          const int i = hp * 2 + t;
          u32x2 B = {pq[i][2 * rt], pq[i][2 * rt + 1]};
          f32x4 z4 = {0.f, 0.f, 0.f, 0.f};
          f32x4 num = mfma16(kv[i], B, z4), den = mfma16(zt[i], B, z4);
          float o[4];
#pragma unroll
          for (int r = 0; r < 4; ++r) o[r] = rbf(num[r]) / rbf(rbf(den[r]) + 1e-6f);
          of[2 * t] = pk2(o[0], o[1]); of[2 * t + 1] = pk2(o[2], o[3]);
        }
        lds_st16(sm, O_OF + ((rt * 16 + 2 * w + hp) * 64 + lane) * 16, of);
      }
  }
  __syncthreads();
  {
    f32x4 a0 = {0.f, 0.f, 0.f, 0.f}, a1 = a0;
#pragma unroll
    for (int ks = 0; ks < 16; ++ks) {
      a0 = mfma32(lds_ld16(sm, O_OF + (ks * 64 + lane) * 16), Wo[ks], a0);
      a1 = mfma32(lds_ld16(sm, O_OF + ((16 + ks) * 64 + lane) * 16), Wo[ks], a1);
    }
    lds_st16(sm, O_T + (16 * w + r16) * 144 + (4 * lq) * 4, __builtin_bit_cast(u32x4, a0));
    lds_st16(sm, O_T + (16 * w + r16) * 144 + (16 + 4 * lq) * 4, __builtin_bit_cast(u32x4, a1));
  }
  __syncthreads();
  f32x4 t0 = __builtin_bit_cast(f32x4, lds_ld16(sm, O_T + ecol * 144 + epc * 32)), t1 = __builtin_bit_cast(f32x4, lds_ld16(sm, O_T + ecol * 144 + epc * 32 + 16));
  u32x4 ov; float xs[8];
#pragma unroll
  for (int j = 0; j < 4; ++j) {
    float a = j < 2 ? t0[2 * j] : t1[2 * j - 4], bq = j < 2 ? t0[2 * j + 1] : t1[2 * j - 3];
    ov[j] = pk2(bflo(xin[j]) + rbf(a), bfhi(xin[j]) + rbf(bq));
    xs[2 * j] = bflo(ov[j]); xs[2 * j + 1] = bfhi(ov[j]);
  }
  if (!FINAL) {
    st16<AUX>(rw, o_xout + xoff, ov);
    float sv[16];
#pragma unroll
    for (int j = 0; j < 8; ++j) { sv[2 * j] = xs[j]; sv[2 * j + 1] = xs[j] * xs[j]; }
#pragma unroll
    for (int o = 4; o < 64; o <<= 1)
#pragma unroll
      for (int j = 0; j < 16; ++j) sv[j] += __shfl_xor(sv[j], o);
    if (lane < 4) {
#pragma unroll
      for (int j = 0; j < 4; ++j) { u32x4 v = {u_(sv[4 * j]), u_(sv[4 * j + 1]), u_(sv[4 * j + 2]), u_(sv[4 * j + 3])}; lds_st16(sm, O_PS + (w * 32 + lane * 8 + 2 * j) * 8, v); }
    }
    __syncthreads();
    if (tid < 32) {
      float s = 0.f, q = 0.f;
#pragma unroll
      for (int ww = 0; ww < 8; ++ww) { u32x2 v = lds_ld8(sm, O_PS + (ww * 32 + tid) * 8); s += f_(v[0]); q += f_(v[1]); }
      u32x2 v = {u_(s), u_(q)};
      st8<AUX>(rw, o_st + (cb4 * L + rb * 32 + tid) * 8, v);
    }
  } else {
    __syncthreads();
#pragma unroll
    for (int j = 0; j < 8; ++j)
      *(LAS unsigned short*)((LAS char*)sm + O_OF + (epc * 8 + j) * 272 + ecol * 2) = (unsigned short)((j & 1) ? (ov[j >> 1] >> 16) : (ov[j >> 1] & 0xffffu));
    __syncthreads();
    const int r = tid >> 4, pc = tid & 15;
    u32x4 v = lds_ld16(sm, O_OF + r * 272 + pc * 16);
    *(u32x4*)(p.out + (size_t)(rb * 32 + r) * 512 + cb4 * 128 + pc * 8) = v;
  }
}
extern __shared__ __attribute__((aligned(16))) char smem[];
__global__ void __launch_bounds__(512, 2) kA(Params p) { phase_A(p, smem); }
template <int NP> __global__ void __launch_bounds__(512, 2) kC(Params p, u32 o_in, u32 o_st, u32 o_out, int pad) {
  rsrc_t rw = mk_rsrc(p.ws, O_END);
  const int b = blockIdx.x, d0 = 2 * ((b & 7) * 32 + (b >> 3));
  conv_build_G(rw, smem, d0);
  phase_C<NP, 0>(p, rw, smem, d0, o_in, o_st, o_out);
}
__global__ void __launch_bounds__(512, 2) kQ(Params p, u32 o_x, int layer) {
  rsrc_t rw = mk_rsrc(p.ws, O_END);
  const int b = blockIdx.x, rb = (b & 7) * 4 + ((b >> 3) & 3), cb = b >> 5;
  u32x4 W[8][3];
  q_load_w(rw, W, cb);
  q_colsum(smem, W);
  phase_Q<0>(rw, smem, W, rb, cb, o_x, O_PQ + layer * XSZ, O_KVP + layer * KVP_SZ, O_ZP + layer * ZP_SZ);
}
__global__ void __launch_bounds__(512, 2) kR(Params p, int layer, int pad) {
  rsrc_t rw = mk_rsrc(p.ws, O_END);
  phase_R<0>(rw, smem, blockIdx.x, O_KVP + layer * KVP_SZ, O_ZP + layer * ZP_SZ, O_KVT + layer * KVT_SZ, O_ZT + layer * ZT_SZ);
}
template <bool FINAL> __global__ void __launch_bounds__(512, 2) kO(Params p, int layer, u32 o_xin, u32 o_xout, u32 o_st) {
  rsrc_t rw = mk_rsrc(p.ws, O_END);
  const int b = blockIdx.x, rb = (b & 7) * 8 + ((b >> 3) & 7), cb4 = b >> 6;
  u32x4 Wo[16];
  o_load_w(rw, Wo, cb4);
  phase_O<0, FINAL>(p, rw, smem, Wo, rb, cb4, O_PQ + layer * XSZ, O_KVT + layer * KVT_SZ, O_ZT + layer * ZT_SZ, o_xin, o_xout, o_st);
}
static Params make_params(void* const* d_in, void* d_out, void* d_ws) {
  Params p;
  p.u = (const u32*)d_in[0]; p.logA = (const unsigned short*)d_in[1]; p.Aim = (const unsigned short*)d_in[2];
  p.Cre = (const unsigned short*)d_in[3]; p.Cim = (const unsigned short*)d_in[4]; p.logdt = (const unsigned short*)d_in[5];
  p.Dskip = (const unsigned short*)d_in[6]; p.wqkv = (const unsigned short*)d_in[8]; p.wo = (const unsigned short*)d_in[9];
  p.ws = (char*)d_ws; p.out = (unsigned short*)d_out;
  return p;
}
extern "C" void kernel_launch(void* const* d_in, const int* in_sizes, int n_in, void* d_out, int out_size, void* d_ws, size_t ws_size,
                              hipStream_t stream) {
  Params p = make_params(d_in, d_out, d_ws);
  (void)hipFuncSetAttribute((const void*)kA, hipFuncAttributeMaxDynamicSharedMemorySize, KA_LDS);
  (void)hipFuncSetAttribute((const void*)kC<1>, hipFuncAttributeMaxDynamicSharedMemorySize, C_LDS);
  (void)hipFuncSetAttribute((const void*)kC<4>, hipFuncAttributeMaxDynamicSharedMemorySize, C_LDS);
  (void)hipFuncSetAttribute((const void*)kQ, hipFuncAttributeMaxDynamicSharedMemorySize, Q_LDS);
  kA<<<NB, NT, KA_LDS, stream>>>(p);
  kC<1><<<NB, NT, C_LDS, stream>>>(p, O_X0, O_ST0, O_X1, 0);
  kQ<<<NB, NT, Q_LDS, stream>>>(p, O_X1, 0);
  kR<<<NB, NT, R_LDS, stream>>>(p, 0, 0);
  kO<false><<<NB, NT, O_LDS, stream>>>(p, 0, O_X1, O_X2, O_ST2);
  kC<4><<<NB, NT, C_LDS, stream>>>(p, O_X2, O_ST2, O_X3, 0);
  kQ<<<NB, NT, Q_LDS, stream>>>(p, O_X3, 1);
  kR<<<NB, NT, R_LDS, stream>>>(p, 1, 0);
  kO<true><<<NB, NT, O_LDS, stream>>>(p, 1, O_X3, 0u, 0u);
}
```

```cpp
#include <hip/hip_runtime.h>
#include <hip/hip_bf16.h>
#include <stdint.h>

typedef unsigned u32;
typedef unsigned u32x4 __attribute__((ext_vector_type(4)));
typedef unsigned u32x2 __attribute__((ext_vector_type(2)));
typedef float f32x4 __attribute__((ext_vector_type(4)));
typedef float f32x2 __attribute__((ext_vector_type(2)));
typedef short s16x4 __attribute__((ext_vector_type(4)));
typedef __bf16 bf16x8 __attribute__((ext_vector_type(8)));
typedef __bf16 bf16x2 __attribute__((ext_vector_type(2)));
#define LAS __attribute__((address_space(3)))
#define DEV __device__ __forceinline__

constexpr int L = 2048, D = 512, NB = 256, NT = 512;
constexpr u32 O_WQ = 0;
constexpr u32 O_WO = O_WQ + 1536 * 512 * 2;
constexpr u32 O_KR = O_WO + 512 * 512 * 2;
constexpr u32 KR_STR = 2064 * 2;
constexpr u32 XSZ = L * D * 2;
constexpr u32 O_X0 = O_KR + 512 * KR_STR;
constexpr u32 O_ST0 = O_X0 + XSZ;
constexpr u32 O_X1 = O_ST0 + L * 8;
constexpr u32 O_X2 = O_X1 + XSZ;
constexpr u32 O_X3 = O_X2 + XSZ;
constexpr u32 O_ST2 = O_X3 + XSZ;
constexpr u32 O_PQ = O_ST2 + 4 * L * 8;
constexpr u32 O_KVP = O_PQ + 2 * XSZ;
constexpr u32 KVP_SZ = 32 * 32 * 1024;
constexpr u32 O_ZP = O_KVP + 2 * KVP_SZ;
constexpr u32 ZP_SZ = 32 * 32 * 64;
constexpr u32 O_KVT = O_ZP + 2 * ZP_SZ;
constexpr u32 KVT_SZ = 32 * 512;
constexpr u32 O_ZT = O_KVT + 2 * KVT_SZ;
constexpr u32 ZT_SZ = 32 * 32;
constexpr u32 O_END = O_ZT + 2 * ZT_SZ;

struct Params {
  const u32* u; const unsigned short* logA; const unsigned short* Aim; const unsigned short* Cre; const unsigned short* Cim;
  const unsigned short* logdt; const unsigned short* Dskip; const unsigned short* wqkv; const unsigned short* wo;
  char* ws; unsigned short* out;
};

DEV float bf2f(u32 h) { return __builtin_bit_cast(float, h << 16); }
DEV float bflo(u32 w) { return __builtin_bit_cast(float, w << 16); }
DEV float bfhi(u32 w) { return __builtin_bit_cast(float, w & 0xffff0000u); }
DEV u32 pk2(float lo, float hi) { f32x2 f = {lo, hi}; return __builtin_bit_cast(u32, __builtin_convertvector(f, bf16x2)); }
DEV float rbf(float x) { return bflo(pk2(x, x)); }
DEV f32x4 mfma32(u32x4 a, u32x4 b, f32x4 c) {
  return __builtin_amdgcn_mfma_f32_16x16x32_bf16(__builtin_bit_cast(bf16x8, a), __builtin_bit_cast(bf16x8, b), c, 0, 0, 0);
}
DEV f32x4 mfma16(u32x2 a, u32x2 b, f32x4 c) {
  return __builtin_amdgcn_mfma_f32_16x16x16bf16_1k(__builtin_bit_cast(s16x4, a), __builtin_bit_cast(s16x4, b), c, 0, 0, 0);
}
DEV float dot2(u32 a, u32 b, float c) { return __builtin_amdgcn_fdot2_f32_bf16(__builtin_bit_cast(bf16x2, a), __builtin_bit_cast(bf16x2, b), c, false); }
typedef __amdgpu_buffer_rsrc_t rsrc_t;
DEV rsrc_t mk_rsrc(const void* p, u32 bytes) { return __builtin_amdgcn_make_buffer_rsrc((void*)p, (short)0, (int)bytes, 0x00020000); }
template <int AUX> DEV u32x4 ld16(rsrc_t r, u32 off) { return __builtin_amdgcn_raw_buffer_load_b128(r, off, 0, AUX); }
template <int AUX> DEV u32x2 ld8(rsrc_t r, u32 off) { return __builtin_amdgcn_raw_buffer_load_b64(r, off, 0, AUX); }
template <int AUX> DEV u32 ld4(rsrc_t r, u32 off) { return __builtin_amdgcn_raw_buffer_load_b32(r, off, 0, AUX); }
template <int AUX> DEV void st16(rsrc_t r, u32 off, u32x4 v) { __builtin_amdgcn_raw_buffer_store_b128(v, r, off, 0, AUX); }
template <int AUX> DEV void st8(rsrc_t r, u32 off, u32x2 v) { __builtin_amdgcn_raw_buffer_store_b64(v, r, off, 0, AUX); }
template <int AUX> DEV void st4(rsrc_t r, u32 off, u32 v) { __builtin_amdgcn_raw_buffer_store_b32(v, r, off, 0, AUX); }
DEV u32x4 lds_ld16(const char* s, u32 off) { return *(const LAS u32x4*)((const LAS char*)s + off); }
DEV u32x2 lds_ld8(const char* s, u32 off) { return *(const LAS u32x2*)((const LAS char*)s + off); }
DEV u32 lds_ld4(const char* s, u32 off) { return *(const LAS u32*)((const LAS char*)s + off); }
DEV void lds_st16(char* s, u32 off, u32x4 v) { *(LAS u32x4*)((LAS char*)s + off) = v; }
DEV void lds_st8(char* s, u32 off, u32x2 v) { *(LAS u32x2*)((LAS char*)s + off) = v; }
DEV void lds_st4(char* s, u32 off, u32 v) { *(LAS u32*)((LAS char*)s + off) = v; }
DEV u32x2 lds_tr(const char* s, u32 off) {
  return __builtin_bit_cast(u32x2, __builtin_amdgcn_ds_read_tr16_b64_v4i16((LAS s16x4*)((LAS char*)s + off)));
}
DEV float f_(u32 x) { return __builtin_bit_cast(float, x); }
DEV u32 u_(float x) { return __builtin_bit_cast(u32, x); }
DEV float xor16(float v) { return __shfl_xor(v, 16); }
DEV float xor32(float v) { return __shfl_xor(v, 32); }
DEV void bar() { asm volatile("s_waitcnt lgkmcnt(0)\n\ts_barrier" ::: "memory"); }
DEV int otid() { int t = threadIdx.x; asm volatile("" : "+v"(t)); return t; }
DEV void sfence() { __builtin_amdgcn_sched_barrier(0); }
constexpr u32 SENT = 0xffffffffu;
constexpr int SC1 = 16;
constexpr int SPIN_CAP = 1 << 14;
DEV bool isbad(u32x4 v) { return max(max(v[0], v[1]), max(v[2], v[3])) == SENT; }
DEV bool isbad(u32x2 v) { return max(v[0], v[1]) == SENT; }
DEV bool anyb(bool b) { return __builtin_amdgcn_ballot_w64(b) != 0ull; }
template <int N, class F> DEV void poll16(rsrc_t r, F off, u32x4 (&v)[N]) {
#pragma unroll
  for (int i = 0; i < N; ++i) v[i] = ld16<SC1>(r, off(i));
  for (int spin = 0; spin < SPIN_CAP; ++spin) {
    u32 mask = 0;
#pragma unroll
    for (int i = 0; i < N; ++i) mask |= anyb(isbad(v[i])) ? (1u << i) : 0u;
    if (!mask) break;
    __builtin_amdgcn_s_sleep(2);
#pragma unroll
    for (int i = 0; i < N; ++i) if (mask & (1u << i)) v[i] = ld16<SC1>(r, off(i));
  }
}
DEV int permk(int g, int j) { return j < 4 ? 4 * g + j : 16 + 4 * g + (j - 4); }
DEV void ln_stats(float sum, float sq, float& m, float& rinv) {
  float mean = sum * (1.0f / 512.0f);
  float var = fmaxf(sq * (1.0f / 512.0f) - mean * mean, 0.0f);
  m = rbf(mean);
  float den = rbf(__builtin_sqrtf(rbf(rbf(var) + 1e-5f)));
  rinv = 1.0f / den;
}
DEV float phi(float t) { float p = t > 0.0f ? t + 1.0f : __expf(t); return rbf(rbf(p) + 1e-6f); }
#define STAMP(i)
#define STAMP_WS(p)
#define STAMP_ARG
#define STAMP_PASS
constexpr u32 KA_ROW = 528;
constexpr u32 KA_TAB = 16 * KA_ROW;
constexpr u32 KA_G = 0, KA_E = 8 * KA_TAB;
constexpr u32 KA_KC = 16 * KA_TAB;
constexpr u32 KA_XT = KA_KC + 2 * 656 * 4;
constexpr u32 KA_LDS = KA_XT + 8 * 1040;
struct cf { float r, i; };
DEV cf cmul(cf a, cf b) { cf c; c.r = a.r * b.r - a.i * b.i; c.i = a.r * b.i + a.i * b.r; return c; }
DEV cf cexp_pow(float are, float aim, int k) {
  float s = (float)(1 << k);
  float mag = __expf(are * s);
  double rev = (double)aim * (0.15915494309189535 * (double)(1 << k));
  float fr = (float)(rev - __builtin_floor(rev));
  cf c; c.r = mag * __builtin_amdgcn_cosf(fr); c.i = mag * __builtin_amdgcn_sinf(fr); return c;
}
DEV void phase_A(const Params& p, char* sm) {
  const int tid = threadIdx.x, b = blockIdx.x, lane = tid & 63, w = tid >> 6;
  rsrc_t rw = mk_rsrc(p.ws, O_END);
  const int rg = (b & 7) * 32 + (b >> 3);
  const int row = rg * 8 + w;
  u32x4 xv = *(const u32x4*)(p.u + (size_t)row * 256 + lane * 4);
  u32 wv[8]; u32 wdst;
  {
    int g = b * NT + tid, ln = g & 63, lq = ln >> 4;
    const unsigned short* src; int ld, ks;
    if (g < 98304) {
      int f = g >> 6, ty = f % 3, f2 = f / 3, ks8 = f2 & 7, kh = (f2 >> 3) & 1, hh = (f2 >> 4) & 3, cb = f2 >> 6;
      ks = kh * 8 + ks8; src = p.wqkv + ty * 512 + (4 * cb + hh) * 16 + (ln & 15); ld = 1536; wdst = O_WQ + (u32)g * 16;
    } else {
      int g2 = g - 98304, f = g2 >> 6; ks = f & 15; int ww = (f >> 4) & 7, cb4 = f >> 7;
      src = p.wo + 128 * cb4 + 16 * ww + (ln & 15); ld = 512; wdst = O_WO + (u32)g2 * 16;
    }
#pragma unroll
    for (int j = 0; j < 8; ++j) wv[j] = src[(size_t)(32 * ks + permk(lq, j)) * ld];
  }
  const int n = tid & 127, ch = (tid >> 7) & 1, sih = tid >> 8;
  const int d = 2 * b + ch;
  float lA = bf2f(p.logA[d * 128 + n]), Ai = bf2f(p.Aim[d * 128 + n]);
  float Cr = bf2f(p.Cre[d * 128 + n]), Ci = bf2f(p.Cim[d * 128 + n]);
  float dt = rbf(expf(bf2f(p.logdt[d])));
  {
    float s = 0.f, q = 0.f;
#pragma unroll
    for (int j = 0; j < 4; ++j) { s = dot2(xv[j], 0x3f803f80u, s); q = dot2(xv[j], xv[j], q); }
#pragma unroll
    for (int o = 1; o < 64; o <<= 1) { s += __shfl_xor(s, o); q += __shfl_xor(q, o); }
    if (lane == 0) { u32x2 v = {u_(s), u_(q)}; st8<0>(rw, O_ST0 + row * 8, v); }
    lds_st16(sm, KA_XT + w * 1040 + lane * 16, xv);
  }
  { u32x4 v = {wv[0] | (wv[1] << 16), wv[2] | (wv[3] << 16), wv[4] | (wv[5] << 16), wv[6] | (wv[7] << 16)}; st16<0>(rw, wdst, v); }
  st16<0>(rw, O_X1 + (u32)(b * NT + tid) * 16, u32x4{SENT, SENT, SENT, SENT});
  float are = -rbf(expf(lA)) * dt, aim = Ai * dt;
  cf C; C.r = Cr * dt; C.i = Ci * dt;
#pragma unroll
  for (int t = 0; t < 2; ++t) {
    const int si = sih + 2 * t;
    const u32 tb = (u32)(ch * 4 + si) * KA_TAB + n * 4;
    cf v0 = cexp_pow(are, aim, si), v1 = cmul(v0, v0), v2 = cmul(v1, v1), v3 = cmul(v2, v2);
    cf e[8]; e[0].r = 1.f; e[0].i = 0.f; e[1] = v0; e[2] = v1; e[3] = cmul(v1, v0); e[4] = v2;
    e[5] = cmul(v2, v0); e[6] = cmul(v2, v1); e[7] = cmul(v2, e[3]);
#pragma unroll
    for (int j = 0; j < 8; ++j) {
      lds_st4(sm, KA_E + tb + j * KA_ROW, pk2(e[j].r, e[j].i));
      cf f = cmul(e[j], v3);
      lds_st4(sm, KA_E + tb + (j + 8) * KA_ROW, pk2(f.r, f.i));
    }
    cf g0 = cexp_pow(are, aim, 4 + si), g1 = cmul(g0, g0), g2 = cmul(g1, g1), g3 = cmul(g2, g2);
    cf h[8]; h[0] = (si == 0) ? C : cmul(C, g3);
    h[1] = cmul(h[0], g0); h[2] = cmul(h[0], g1); h[3] = cmul(h[1], g1);
#pragma unroll
    for (int j = 0; j < 4; ++j) h[4 + j] = cmul(h[j], g2);
#pragma unroll
    for (int j = 0; j < 8; ++j) {
      lds_st4(sm, KA_G + tb + j * KA_ROW, pk2(h[j].r, -h[j].i));
      cf f = cmul(h[j], g3);
      lds_st4(sm, KA_G + tb + (j + 8) * KA_ROW, pk2(f.r, -f.i));
    }
  }
  __syncthreads();
  {
    u32 v[8];
#pragma unroll
    for (int r = 0; r < 8; ++r) v[r] = *(const LAS unsigned short*)((LAS char*)sm + KA_XT + r * 1040 + tid * 2);
    u32x4 o = {v[0] | (v[1] << 16), v[2] | (v[3] << 16), v[4] | (v[5] << 16), v[6] | (v[7] << 16)};
    st16<0>(rw, O_X0 + (((rg >> 2) * 512 + tid) * 32 + (rg & 3) * 8) * 2, o);
  }
  {
    const int wc = w >> 2, si = w & 3, lq = lane >> 4, r16 = lane & 15;
    const u32 base = (u32)(wc * 4 + si) * KA_TAB + r16 * KA_ROW + lq * 16;
    f32x4 acc = {0.f, 0.f, 0.f, 0.f};
#pragma unroll
    for (int ks = 0; ks < 8; ++ks) acc = mfma32(lds_ld16(sm, KA_G + base + ks * 64), lds_ld16(sm, KA_E + base + ks * 64), acc);
#pragma unroll
    for (int r = 0; r < 4; ++r) {
      int i = 4 * lq + r;
      int idx = si == 0 ? 16 * i + r16 : 256 + (si - 1) * 128 + 16 * i + r16;
      bool ok = si == 0 || i < 8 || (si == 3 && i == 8 && r16 == 0);
      if (ok) lds_st4(sm, KA_KC + (wc * 656 + idx) * 4, u_(2.0f * acc[r]));
    }
  }
  __syncthreads();
  {
    const int c2 = tid >> 8, m = tid & 255;
    u32 o[4];
#pragma unroll
    for (int j = 0; j < 4; ++j) {
      float kv[2];
#pragma unroll
      for (int h = 0; h < 2; ++h) {
        int l = 2047 - (8 * m + 2 * j + h);
        int e = l < 256 ? 0 : (l < 512 ? 1 : (l < 1024 ? 2 : 3));
        int mm = (l + ((1 << e) >> 1) - 1 + ((l >> e) & 1)) >> e;
        int idx = e == 0 ? l : 256 + (e - 1) * 128 + (mm - 128);
        kv[h] = f_(lds_ld4(sm, KA_KC + (c2 * 656 + idx) * 4));
      }
      o[j] = pk2(kv[0], kv[1]);
    }
    u32x4 ov = {o[0], o[1], o[2], o[3]};
    const u32 kb = O_KR + (u32)(2 * b + c2) * KR_STR;
    st16<0>(rw, kb + m * 16, ov);
    if (m < 2) { u32x4 z = {0u, 0u, 0u, 0u}; st16<0>(rw, kb + 4096 + m * 16, z); }
  }
}
constexpr u32 P_G = 0, P_GSTR = 2064 * 16;
constexpr u32 DB = 2 * P_GSTR;
constexpr u32 P_CS = 158720;
constexpr u32 P_LDS = P_CS + 8 * 3 * 16 * 4;
constexpr u32 C_U = DB, C_ST = C_U + 2 * 4608, C_RED = C_ST + L * 8;
constexpr u32 Q_A = DB, Q_EX = DB, Q_SP = DB + 65536, Q_MR = Q_SP + 1024;
constexpr u32 O_OF = DB, O_T = DB + 32768, O_PS = O_T + 128 * 144;
constexpr u32 R_B = DB;
static_assert(C_RED + 65536 <= P_CS && Q_MR + 8 * 512 <= P_CS && P_LDS <= 163840, "LDS map");

DEV void fill16(rsrc_t rw, u32 off) { st16<SC1>(rw, off, u32x4{SENT, SENT, SENT, SENT}); }

DEV void conv_build_G(rsrc_t rw, char* sm, int d0) {
  const int tid = otid(), ch = tid >> 8, y0 = tid & 255;
  const u32 kb = O_KR + (u32)(d0 + ch) * KR_STR + y0 * 4;
  u32x4 a[4]; u32 e[4];
#pragma unroll
  for (int i = 0; i < 4; ++i) { a[i] = ld16<0>(rw, kb + i * 1024); e[i] = ld4<0>(rw, kb + i * 1024 + 16); }
#pragma unroll
  for (int i = 0; i < 4; ++i) {
    const u32 gb = P_G + ch * P_GSTR + (y0 + 256 * i) * 32;
    lds_st16(sm, gb, a[i]);
    u32x4 o = {__builtin_amdgcn_alignbit(a[i][1], a[i][0], 16), __builtin_amdgcn_alignbit(a[i][2], a[i][1], 16),
               __builtin_amdgcn_alignbit(a[i][3], a[i][2], 16), __builtin_amdgcn_alignbit(e[i], a[i][3], 16)};
    lds_st16(sm, gb + 16, o);
  }
  if (y0 < 16) lds_st16(sm, P_G + ch * P_GSTR + (2048 + y0) * 16, u32x4{0u, 0u, 0u, 0u});
}

DEV void q_load_w(rsrc_t rw, u32x4 (&W)[8][3], int cb) {
  const int tid = otid(), lane = tid & 63, w = tid >> 6;
  const u32 base = O_WQ + (u32)(cb * 8 + w) * 24576 + lane * 16;
#pragma unroll
  for (int ks = 0; ks < 8; ++ks)
#pragma unroll
    for (int ty = 0; ty < 3; ++ty) W[ks][ty] = ld16<0>(rw, base + (ks * 3 + ty) * 1024);
}
template <int NP, bool POLL>
DEV void phase_C(rsrc_t rw, char* sm, u32x4 (&W)[8][3], int cb, int d0, float dsk, u32 o_in, u32 o_st, u32 o_out, int sb STAMP_ARG) {
  const int tid = otid(), lane = tid & 63, w = tid >> 6, ch = tid >> 8, m = tid & 255;
  const u32 xoff = (((m >> 2) * 512 + d0 + ch) * 32 + (m & 3) * 8) * 2;
  u32x4 xv;
  {
    u32x4 sv[2 * NP + 1];
    auto off = [&](int i) -> u32 { return i == 2 * NP ? o_in + xoff : o_st + (i >> 1) * L * 8 + tid * 32 + (i & 1) * 16; };
    if (POLL) poll16<2 * NP + 1>(rw, off, sv);
    else {
#pragma unroll
      for (int i = 0; i < 2 * NP + 1; ++i) sv[i] = ld16<0>(rw, off(i));
    }
    xv = sv[2 * NP];
    sfence();
    q_load_w(rw, W, cb);
    sfence();
    float mr[8];
#pragma unroll
    for (int r = 0; r < 4; ++r) {
      float s = 0.f, q2 = 0.f;
#pragma unroll
      for (int q = 0; q < NP; ++q) { s += f_(sv[2 * q + (r >> 1)][(r & 1) * 2]); q2 += f_(sv[2 * q + (r >> 1)][(r & 1) * 2 + 1]); }
      ln_stats(s, q2, mr[2 * r], mr[2 * r + 1]);
    }
    lds_st16(sm, C_ST + tid * 32, u32x4{u_(mr[0]), u_(mr[1]), u_(mr[2]), u_(mr[3])});
    lds_st16(sm, C_ST + tid * 32 + 16, u32x4{u_(mr[4]), u_(mr[5]), u_(mr[6]), u_(mr[7])});
    if (tid < 64) lds_st16(sm, C_U + (tid >> 5) * 4608 + ((tid >> 4) & 1) * 2304 + (tid & 15) * 16, u32x4{0u, 0u, 0u, 0u});
  }
  bar();
  STAMP(sb + 1);
  u32x4 uv;
#pragma unroll
  for (int j = 0; j < 4; ++j) {
    u32x4 s = lds_ld16(sm, C_ST + (8 * m + 2 * j) * 8);
    uv[j] = pk2(rbf(bflo(xv[j]) - f_(s[0])) * f_(s[1]), rbf(bfhi(xv[j]) - f_(s[2])) * f_(s[3]));
  }
  lds_st16(sm, C_U + ch * 4608 + (m & 1) * 2304 + (16 + (m >> 1)) * 16, uv);
  bar();
  {
    const int wc = w >> 2, w4 = w & 3, lq = lane >> 4, r16 = lane & 15;
    const int j0 = (lq & 1) * 16 + (lq >> 1) * 8, rho = 2 * w4;
    const u32 ub = C_U + wc * 4608 + (lq >> 1) * 2304 + (15 - 2 * rho + r16 + (lq & 1)) * 16;
    const u32 gb = P_G + wc * P_GSTR + (2031 - 32 * (rho + 1) - 256 * 7 - r16 + j0) * 16;
    f32x4 acc[8];
#pragma unroll
    for (int c = 0; c < 8; ++c) acc[c] = f32x4{0.f, 0.f, 0.f, 0.f};
    u32x4 B0[8], A0[8], B1[8], A1[8];
#pragma unroll
    for (int k = 0; k < 8; ++k) { A0[k] = lds_ld16(sm, gb + 512 + (7 - k) * 4096); B0[k] = lds_ld16(sm, ub + k * 256); }
    sfence();
#pragma unroll
    for (int g = 0; g < 8; ++g) {
      A1[g] = lds_ld16(sm, gb + (7 - g) * 4096); B1[g] = lds_ld16(sm, ub - 32 + g * 256);
      sfence();
#pragma unroll
      for (int c = 7; c >= g; --c) acc[c] = mfma32(A0[g], B0[c - g], acc[c]);
      sfence();
    }
#pragma unroll
    for (int g = 0; g < 8; ++g) {
#pragma unroll
      for (int c = g; c < 8; ++c) acc[c] = mfma32(A1[g], B1[c - g], acc[c]);
    }
#pragma unroll
    for (int c = 0; c < 8; ++c) lds_st16(sm, C_RED + ((wc * 4 + w4) * 8 + c) * 1024 + lane * 16, __builtin_bit_cast(u32x4, acc[c]));
  }
  if (!POLL) asm volatile("s_waitcnt vmcnt(0)" ::: "memory");
  bar();
  STAMP(sb + 2);
  {
    const int c = m >> 5, a = (m & 31) >> 1, lq0 = (m & 1) * 2;
    f32x4 y0 = {0.f, 0.f, 0.f, 0.f}, y1 = y0;
#pragma unroll
    for (int q = 0; q < 4; ++q) {
      const u32 o = C_RED + ((ch * 4 + q) * 8 + c) * 1024 + (lq0 * 16 + a) * 16;
      y0 += __builtin_bit_cast(f32x4, lds_ld16(sm, o)); y1 += __builtin_bit_cast(f32x4, lds_ld16(sm, o + 256));
    }
    u32x4 ov;
#pragma unroll
    for (int j = 0; j < 4; ++j) {
      float ya = j < 2 ? y0[2 * j] : y1[2 * j - 4], yb = j < 2 ? y0[2 * j + 1] : y1[2 * j - 3];
      float ra = rbf(rbf(ya) + rbf(bflo(uv[j]) * dsk)), rb = rbf(rbf(yb) + rbf(bfhi(uv[j]) * dsk));
      ov[j] = pk2(bflo(xv[j]) + ra, bfhi(xv[j]) + rb);
    }
    st16<SC1>(rw, o_out + xoff, ov);
  }
  bar();
}

DEV void q_colsum(char* sm, const u32x4 (&W)[8][3]) {
  const int tid = otid(), lane = tid & 63, w = tid >> 6;
#pragma unroll
  for (int ty = 0; ty < 3; ++ty) {
    float s = 0.f;
#pragma unroll
    for (int ks = 0; ks < 8; ++ks)
#pragma unroll
      for (int j = 0; j < 4; ++j) s = dot2(W[ks][ty][j], 0x3f803f80u, s);
    s += xor16(s); s += xor32(s);
    if (lane < 16) lds_st4(sm, P_CS + ((w * 3 + ty) * 16 + lane) * 4, u_(s));
  }
}
DEV void o_load_w(rsrc_t rw, u32x4 (&Wo)[16], int cb4) {
  const int tid = otid(), lane = tid & 63, w = tid >> 6;
#pragma unroll
  for (int ks = 0; ks < 16; ++ks) Wo[ks] = ld16<0>(rw, O_WO + ((cb4 * 8 + w) * 16 + ks) * 1024 + lane * 16);
}

DEV void phase_Q(rsrc_t rw, char* sm, const u32x4 (&W)[8][3], u32x4 (&Wo)[16], int rb, int cb, int cb4, u32 o_x, u32 o_pq, u32 o_kvp, u32 o_zp,
                 int sb STAMP_ARG) {
  const int tid = otid(), lane = tid & 63, w = tid >> 6, hh = w >> 1, kh = w & 1, lq = lane >> 4, r16 = lane & 15;
  {
    u32x4 av[8];
    poll16<8>(rw, [&](int i) -> u32 { return o_x + (u32)rb * 65536 + (tid + i * 512) * 16; }, av);
    const int chn = tid >> 2, pc = tid & 3, f = (chn >> 1) & 3;
#pragma unroll
    for (int i = 0; i < 8; ++i)
      lds_st16(sm, Q_A + (chn + 128 * (i & 3)) * 128 + ((((i >> 2) * 2 + (pc >> 1)) ^ f) * 32) + (pc & 1) * 16, av[i]);
  }
  bar();
  STAMP(sb + 1);
  f32x4 acc[4][3];
#pragma unroll
  for (int rt = 0; rt < 4; ++rt)
#pragma unroll
    for (int ty = 0; ty < 3; ++ty) acc[rt][ty] = f32x4{0.f, 0.f, 0.f, 0.f};
  const int chl = 4 * lq + (r16 >> 2), f = (chl >> 1) & 3;
  u32 ab[4];
#pragma unroll
  for (int rt = 0; rt < 4; ++rt) ab[rt] = Q_A + (kh * 256 + chl) * 128 + ((rt ^ f) * 32) + (r16 & 3) * 8;
  const u32 abs_ = Q_A + (kh * 256 + chl) * 128 + ((hh ^ f) * 32) + (r16 & 3) * 8;
  float ssum = 0.f, ssq = 0.f;
  {
    u32x2 xa[2][4][2], xs[2][2];
#pragma unroll
    for (int rt = 0; rt < 4; ++rt) { xa[0][rt][0] = lds_tr(sm, ab[rt]); xa[0][rt][1] = lds_tr(sm, ab[rt] + 2048); }
    xs[0][0] = lds_tr(sm, abs_); xs[0][1] = lds_tr(sm, abs_ + 2048);
#pragma unroll
    for (int ks = 0; ks < 8; ++ks) {
      const int cu = ks & 1, nx = cu ^ 1;
      if (ks < 7) {
#pragma unroll
        for (int rt = 0; rt < 4; ++rt) { xa[nx][rt][0] = lds_tr(sm, ab[rt] + (ks + 1) * 4096); xa[nx][rt][1] = lds_tr(sm, ab[rt] + (ks + 1) * 4096 + 2048); }
        xs[nx][0] = lds_tr(sm, abs_ + (ks + 1) * 4096); xs[nx][1] = lds_tr(sm, abs_ + (ks + 1) * 4096 + 2048);
      }
      sfence();
#pragma unroll
      for (int rt = 0; rt < 4; ++rt) {
        u32x4 X = {xa[cu][rt][0][0], xa[cu][rt][0][1], xa[cu][rt][1][0], xa[cu][rt][1][1]};
        acc[rt][0] = mfma32(W[ks][0], X, acc[rt][0]);
        acc[rt][1] = mfma32(X, W[ks][1], acc[rt][1]);
        acc[rt][2] = mfma32(X, W[ks][2], acc[rt][2]);
      }
#pragma unroll
      for (int j = 0; j < 2; ++j)
#pragma unroll
        for (int e = 0; e < 2; ++e) { ssum = dot2(xs[cu][j][e], 0x3f803f80u, ssum); ssq = dot2(xs[cu][j][e], xs[cu][j][e], ssq); }
      sfence();
    }
  }
  ssum += xor16(ssum); ssum += xor32(ssum); ssq += xor16(ssq); ssq += xor32(ssq);
  if (lane < 16) lds_st8(sm, Q_SP + (kh * 64 + hh * 16 + lane) * 8, u32x2{u_(ssum), u_(ssq)});
  bar();
  o_load_w(rw, Wo, cb4);
  const u32 ex = Q_EX + hh * 12288 + lane * 16;
  if (kh == 0) {
#pragma unroll
    for (int rt = 0; rt < 4; ++rt) lds_st16(sm, ex + rt * 1024, __builtin_bit_cast(u32x4, acc[rt][0]));
  } else {
#pragma unroll
    for (int rt = 0; rt < 4; ++rt) {
      lds_st16(sm, ex + (4 + rt * 2) * 1024, __builtin_bit_cast(u32x4, acc[rt][1]));
      lds_st16(sm, ex + (5 + rt * 2) * 1024, __builtin_bit_cast(u32x4, acc[rt][2]));
    }
  }
  {
    u32x2 p0 = lds_ld8(sm, Q_SP + lane * 8), p1 = lds_ld8(sm, Q_SP + (64 + lane) * 8);
    float mean, rinv; ln_stats(f_(p0[0]) + f_(p1[0]), f_(p0[1]) + f_(p1[1]), mean, rinv);
    lds_st8(sm, Q_MR + w * 512 + lane * 8, u32x2{u_(mean), u_(rinv)});
  }
  bar();
  STAMP(sb + 2);
  const int h = cb * 4 + hh;
  const u32 mrb = Q_MR + w * 512, csb = P_CS + hh * 2 * 3 * 16 * 4;
  if (kh == 1) {
    const f32x4 cs = __builtin_bit_cast(f32x4, lds_ld16(sm, csb + 16 * lq)) + __builtin_bit_cast(f32x4, lds_ld16(sm, csb + 192 + 16 * lq));
#pragma unroll
    for (int rp = 0; rp < 2; ++rp) {
      u32x4 ov;
#pragma unroll
      for (int t = 0; t < 2; ++t) {
        const int rt = rp * 2 + t;
        f32x4 a = acc[rt][0] + __builtin_bit_cast(f32x4, lds_ld16(sm, ex + rt * 1024));
        u32x2 mr = lds_ld8(sm, mrb + (rt * 16 + r16) * 8);
        float qv[4];
#pragma unroll
        for (int r = 0; r < 4; ++r) qv[r] = phi(rbf((a[r] - f_(mr[0]) * cs[r]) * f_(mr[1])));
        ov[2 * t] = pk2(qv[0], qv[1]); ov[2 * t + 1] = pk2(qv[2], qv[3]);
      }
      st16<SC1>(rw, o_pq + (((rb * 2 + rp) * 32 + h) * 64 + lane) * 16, ov);
    }
  } else {
    const float csk = f_(lds_ld4(sm, csb + 64 + r16 * 4)) + f_(lds_ld4(sm, csb + 192 + 64 + r16 * 4));
    const float csv = f_(lds_ld4(sm, csb + 128 + r16 * 4)) + f_(lds_ld4(sm, csb + 192 + 128 + r16 * 4));
    f32x4 kv = {0.f, 0.f, 0.f, 0.f}, zz = kv;
#pragma unroll
    for (int rp = 0; rp < 2; ++rp) {
      u32x4 pk, vv;
#pragma unroll
      for (int t = 0; t < 2; ++t) {
        const int rt = rp * 2 + t;
        f32x4 ak = acc[rt][1] + __builtin_bit_cast(f32x4, lds_ld16(sm, ex + (4 + rt * 2) * 1024));
        f32x4 av = acc[rt][2] + __builtin_bit_cast(f32x4, lds_ld16(sm, ex + (5 + rt * 2) * 1024));
        u32x4 m0 = lds_ld16(sm, mrb + (rt * 16 + 4 * lq) * 8), m1 = lds_ld16(sm, mrb + (rt * 16 + 4 * lq + 2) * 8);
        float kf[4], vf[4];
#pragma unroll
        for (int r = 0; r < 4; ++r) {
          const float mean = f_(r < 2 ? m0[2 * r] : m1[2 * r - 4]), rinv = f_(r < 2 ? m0[2 * r + 1] : m1[2 * r - 3]);
          kf[r] = phi(rbf((ak[r] - mean * csk) * rinv));
          vf[r] = (av[r] - mean * csv) * rinv;
        }
        pk[2 * t] = pk2(kf[0], kf[1]); pk[2 * t + 1] = pk2(kf[2], kf[3]);
        vv[2 * t] = pk2(vf[0], vf[1]); vv[2 * t + 1] = pk2(vf[2], vf[3]);
      }
      kv = mfma32(pk, vv, kv);
      zz = mfma32(pk, u32x4{0x3f803f80u, 0x3f803f80u, 0x3f803f80u, 0x3f803f80u}, zz);
    }
    st16<SC1>(rw, o_kvp + ((rb * 32 + h) * 64 + lane) * 16, __builtin_bit_cast(u32x4, kv));
    if (r16 == 0) st16<SC1>(rw, o_zp + (rb * 32 + h) * 64 + lq * 16, __builtin_bit_cast(u32x4, zz));
  }
  bar();
}

DEV void phase_R(rsrc_t rw, char* sm, int b, u32 o_kvp, u32 o_zp, u32 o_kvt, u32 o_zt) {
  const int tid = otid(), h = b >> 3, j = b & 7, rb = tid >> 4, q = tid & 15;
  u32x4 v[1];
  const u32 off = q < 8 ? o_kvp + ((rb * 32 + h) * 64 + 8 * j + q) * 16 : o_zp + (rb * 32 + h) * 64 + (j >> 1) * 16;
  poll16<1>(rw, [&](int) -> u32 { return off; }, v);
  if (q < 8) lds_st16(sm, R_B + (rb * 40 + q * 4) * 4, v[0]);
  else if (q == 8) lds_st8(sm, R_B + (rb * 40 + 32) * 4, (j & 1) ? u32x2{v[0][2], v[0][3]} : u32x2{v[0][0], v[0][1]});
  bar();
  if (tid < 64) {
    const int e = tid < 34 ? tid : 0;
    float s = 0.f;
#pragma unroll
    for (int r = 0; r < 32; ++r) s += f_(lds_ld4(sm, R_B + (r * 40 + e) * 4));
    float o = __shfl_down(s, 1), o2 = __shfl_down(s, 2), o3 = __shfl_down(s, 3);
    if (tid < 32 && (tid & 3) == 0) st8<SC1>(rw, o_kvt + (h * 64 + 8 * j + (tid >> 2)) * 8, u32x2{pk2(s, o), pk2(o2, o3)});
    if (tid == 32) st4<SC1>(rw, o_zt + h * 32 + j * 4, pk2(s, o));
  }
  bar();
}

template <bool FINAL>
DEV void phase_O(unsigned short* out, rsrc_t rw, char* sm, const u32x4 (&Wo)[16], int rb, int cb4, u32 o_pq, u32 o_kvt, u32 o_zt,
                 u32 o_xin, u32 o_xout, u32 o_st, int sb STAMP_ARG) {
  const int tid = otid(), lane = tid & 63, w = tid >> 6, lq = lane >> 4, r16 = lane & 15;
  const int ecol = tid >> 2, epc = tid & 3;
  const u32 xoff = ((rb * 512 + cb4 * 128 + ecol) * 32 + epc * 8) * 2;
  u32x4 xin;
  {
    u32x4 v[5];
    poll16<5>(rw, [&](int i) -> u32 { return i < 4 ? o_pq + ((rb * 32 + 4 * w + i) * 64 + lane) * 16 : o_xin + xoff; }, v);
    xin = v[4];
    u32x2 kv[4], zt[4];
#pragma unroll
    for (int i = 0; i < 4; ++i) { kv[i] = ld8<SC1>(rw, o_kvt + ((4 * w + i) * 64 + lane) * 8); zt[i] = ld8<SC1>(rw, o_zt + (4 * w + i) * 32 + lq * 8); }
    for (int spin = 0; spin < SPIN_CAP; ++spin) {
      bool bad = false;
#pragma unroll
      for (int i = 0; i < 4; ++i) bad = bad || isbad(kv[i]) || isbad(zt[i]);
      if (!anyb(bad)) break;
      __builtin_amdgcn_s_sleep(1);
#pragma unroll
      for (int i = 0; i < 4; ++i) { kv[i] = ld8<SC1>(rw, o_kvt + ((4 * w + i) * 64 + lane) * 8); zt[i] = ld8<SC1>(rw, o_zt + (4 * w + i) * 32 + lq * 8); }
    }
    STAMP(sb + 1);
#pragma unroll
    for (int hp = 0; hp < 2; ++hp)
#pragma unroll
      for (int rt = 0; rt < 2; ++rt) {
        u32x4 of;
#pragma unroll
        for (int t = 0; t < 2; ++t) {
          const int i = hp * 2 + t;
          u32x2 B = {v[i][2 * rt], v[i][2 * rt + 1]};
          f32x4 z4 = {0.f, 0.f, 0.f, 0.f};
          f32x4 num = mfma16(kv[i], B, z4), den = mfma16(zt[i], B, z4);
          float o[4];
#pragma unroll
          for (int r = 0; r < 4; ++r) o[r] = rbf(num[r]) * __builtin_amdgcn_rcpf(rbf(rbf(den[r]) + 1e-6f));
          of[2 * t] = pk2(o[0], o[1]); of[2 * t + 1] = pk2(o[2], o[3]);
        }
        lds_st16(sm, O_OF + ((rt * 16 + 2 * w + hp) * 64 + lane) * 16, of);
      }
  }
  bar();
  {
    f32x4 a0 = {0.f, 0.f, 0.f, 0.f}, a1 = a0;
    u32x4 o0[16], o1[16];
#pragma unroll
    for (int ks = 0; ks < 4; ++ks) { o0[ks] = lds_ld16(sm, O_OF + (ks * 64 + lane) * 16); o1[ks] = lds_ld16(sm, O_OF + ((16 + ks) * 64 + lane) * 16); }
#pragma unroll
    for (int ks = 0; ks < 16; ++ks) {
      if (ks + 4 < 16) { o0[ks + 4] = lds_ld16(sm, O_OF + ((ks + 4) * 64 + lane) * 16); o1[ks + 4] = lds_ld16(sm, O_OF + ((20 + ks) * 64 + lane) * 16); }
      sfence();
      a0 = mfma32(o0[ks], Wo[ks], a0);
      a1 = mfma32(o1[ks], Wo[ks], a1);
      sfence();
    }
    lds_st16(sm, O_T + (16 * w + r16) * 144 + (4 * lq) * 4, __builtin_bit_cast(u32x4, a0));
    lds_st16(sm, O_T + (16 * w + r16) * 144 + (16 + 4 * lq) * 4, __builtin_bit_cast(u32x4, a1));
  }
  bar();
  STAMP(sb + 2);
  f32x4 t0 = __builtin_bit_cast(f32x4, lds_ld16(sm, O_T + ecol * 144 + epc * 32)), t1 = __builtin_bit_cast(f32x4, lds_ld16(sm, O_T + ecol * 144 + epc * 32 + 16));
  u32x4 ov;
#pragma unroll
  for (int j = 0; j < 4; ++j) {
    float a = j < 2 ? t0[2 * j] : t1[2 * j - 4], bq = j < 2 ? t0[2 * j + 1] : t1[2 * j - 3];
    ov[j] = pk2(bflo(xin[j]) + rbf(a), bfhi(xin[j]) + rbf(bq));
  }
  if (!FINAL) {
    st16<SC1>(rw, o_xout + xoff, ov);
#pragma unroll
    for (int j = 0; j < 8; ++j)
      *(LAS unsigned short*)((LAS char*)sm + O_OF + (epc * 8 + j) * 272 + ecol * 2) = (unsigned short)((j & 1) ? (ov[j >> 1] >> 16) : (ov[j >> 1] & 0xffffu));
    bar();
    const int r = tid >> 4, pc = tid & 15;
    u32x4 v = lds_ld16(sm, O_OF + r * 272 + pc * 16);
    float s = 0.f, q = 0.f;
#pragma unroll
    for (int j = 0; j < 4; ++j) { s = dot2(v[j], 0x3f803f80u, s); q = dot2(v[j], v[j], q); }
#pragma unroll
    for (int o = 1; o < 16; o <<= 1) { s += __shfl_xor(s, o); q += __shfl_xor(q, o); }
    float s1 = __shfl_down(s, 16), q1 = __shfl_down(q, 16);
    if ((lane & 31) == 0) st16<SC1>(rw, o_st + (cb4 * L + rb * 32 + r) * 8, u32x4{u_(s), u_(q), u_(s1), u_(q1)});
    bar();
  } else {
#pragma unroll
    for (int j = 0; j < 8; ++j)
      *(LAS unsigned short*)((LAS char*)sm + O_OF + (epc * 8 + j) * 272 + ecol * 2) = (unsigned short)((j & 1) ? (ov[j >> 1] >> 16) : (ov[j >> 1] & 0xffffu));
    bar();
    const int r = tid >> 4, pc = tid & 15;
    *(u32x4*)(out + (size_t)(rb * 32 + r) * 512 + cb4 * 128 + pc * 8) = lds_ld16(sm, O_OF + r * 272 + pc * 16);
  }
}

extern __shared__ __attribute__((aligned(16))) char smem[];
__global__ void __launch_bounds__(512, 2) kA(Params p) { STAMP_WS(p); STAMP(0); phase_A(p, smem); STAMP(1); }
__global__ void __launch_bounds__(512, 2) kB(Params p) {
  STAMP_WS(p); STAMP(2);
  rsrc_t rw = mk_rsrc(p.ws, O_END);
  const int b = blockIdx.x, tid = otid();
  const int d0 = 2 * ((b & 7) * 32 + (b >> 3));
  const int rbq = (b & 7) * 4 + ((b >> 3) & 3), cb = b >> 5;
  const int rbo = (b & 7) * 8 + ((b >> 3) & 7), cb4 = b >> 6;
  const float dsk = bf2f(p.Dskip[d0 + (tid >> 8)]);
  conv_build_G(rw, smem, d0);
  u32x4 W[8][3], Wo[16];
  {
    const u32 g = (u32)(b * NT + tid) * 16;
    fill16(rw, O_X2 + g); fill16(rw, O_X3 + g); fill16(rw, O_PQ + g); fill16(rw, O_PQ + XSZ + g); fill16(rw, O_KVP + g);
    constexpr u32 rest = O_END - O_ZP;
    if (g < rest) fill16(rw, O_ZP + g);
    if (g < 4 * L * 8) fill16(rw, O_ST2 + g);
  }
  phase_C<1, false>(rw, smem, W, cb, d0, dsk, O_X0, O_ST0, O_X1, 3 STAMP_PASS);
  q_colsum(smem, W);
  STAMP(6);
  phase_Q(rw, smem, W, Wo, rbq, cb, cb4, O_X1, O_PQ, O_KVP, O_ZP, 6 STAMP_PASS);
  STAMP(9);
  phase_R(rw, smem, b, O_KVP, O_ZP, O_KVT, O_ZT);
  STAMP(10);
  phase_O<false>(p.out, rw, smem, Wo, rbo, cb4, O_PQ, O_KVT, O_ZT, O_X1, O_X2, O_ST2, 10 STAMP_PASS);
  STAMP(13);
  phase_C<4, true>(rw, smem, W, cb, d0, dsk, O_X2, O_ST2, O_X3, 13 STAMP_PASS);
  STAMP(16);
  phase_Q(rw, smem, W, Wo, rbq, cb, cb4, O_X3, O_PQ + XSZ, O_KVP + KVP_SZ, O_ZP + ZP_SZ, 16 STAMP_PASS);
  STAMP(19);
  phase_R(rw, smem, b, O_KVP + KVP_SZ, O_ZP + ZP_SZ, O_KVT + KVT_SZ, O_ZT + ZT_SZ);
  STAMP(20);
  phase_O<true>(p.out, rw, smem, Wo, rbo, cb4, O_PQ + XSZ, O_KVT + KVT_SZ, O_ZT + ZT_SZ, O_X3, 0u, 0u, 20 STAMP_PASS);
  STAMP(23);
}
static Params make_params(void* const* d_in, void* d_out, void* d_ws) {
  Params p;
  p.u = (const u32*)d_in[0]; p.logA = (const unsigned short*)d_in[1]; p.Aim = (const unsigned short*)d_in[2];
  p.Cre = (const unsigned short*)d_in[3]; p.Cim = (const unsigned short*)d_in[4]; p.logdt = (const unsigned short*)d_in[5];
  p.Dskip = (const unsigned short*)d_in[6]; p.wqkv = (const unsigned short*)d_in[8]; p.wo = (const unsigned short*)d_in[9];
  p.ws = (char*)d_ws; p.out = (unsigned short*)d_out;
  return p;
}
extern "C" void kernel_launch(void* const* d_in, const int* in_sizes, int n_in, void* d_out, int out_size, void* d_ws, size_t ws_size,
                              hipStream_t stream) {
  Params p = make_params(d_in, d_out, d_ws);
  (void)hipFuncSetAttribute((const void*)kA, hipFuncAttributeMaxDynamicSharedMemorySize, KA_LDS);
  (void)hipFuncSetAttribute((const void*)kB, hipFuncAttributeMaxDynamicSharedMemorySize, P_LDS);
  kA<<<NB, NT, KA_LDS, stream>>>(p);
  kB<<<NB, NT, P_LDS, stream>>>(p);
}
```

```cpp
#include <hip/hip_runtime.h>
#include <hip/hip_bf16.h>
#include <stdint.h>

typedef unsigned u32;
typedef unsigned u32x4 __attribute__((ext_vector_type(4)));
typedef unsigned u32x2 __attribute__((ext_vector_type(2)));
typedef float f32x4 __attribute__((ext_vector_type(4)));
typedef float f32x2 __attribute__((ext_vector_type(2)));
typedef short s16x4 __attribute__((ext_vector_type(4)));
typedef __bf16 bf16x8 __attribute__((ext_vector_type(8)));
typedef __bf16 bf16x2 __attribute__((ext_vector_type(2)));
#define LAS __attribute__((address_space(3)))
#define DEV __device__ __forceinline__

constexpr int L = 2048, D = 512, NB = 256, NT = 512;
constexpr u32 O_WQ = 0;
constexpr u32 O_WO = O_WQ + 1536 * 512 * 2;
constexpr u32 O_KR = O_WO + 512 * 512 * 2;
constexpr u32 KR_STR = 2064 * 2;
constexpr u32 XSZ = L * D * 2;
constexpr u32 O_X0 = O_KR + 512 * KR_STR;
constexpr u32 O_ST0 = O_X0 + XSZ;
constexpr u32 O_X1 = O_ST0 + L * 8;
constexpr u32 O_X2 = O_X1 + XSZ;
constexpr u32 O_X3 = O_X2 + XSZ;
constexpr u32 O_ST2 = O_X3 + XSZ;
constexpr u32 O_PQ = O_ST2 + 4 * L * 8;
constexpr u32 O_KVP = O_PQ + 2 * XSZ;
constexpr u32 KVP_SZ = 32 * 32 * 1024;
constexpr u32 O_ZP = O_KVP + 2 * KVP_SZ;
constexpr u32 ZP_SZ = 32 * 32 * 64;
constexpr u32 O_KVT = O_ZP + 2 * ZP_SZ;
constexpr u32 KVT_SZ = 32 * 512;
constexpr u32 O_ZT = O_KVT + 2 * KVT_SZ;
constexpr u32 ZT_SZ = 32 * 32;
constexpr u32 O_END = O_ZT + 2 * ZT_SZ;

struct Params {
  const u32* u; const unsigned short* logA; const unsigned short* Aim; const unsigned short* Cre; const unsigned short* Cim;
  const unsigned short* logdt; const unsigned short* Dskip; const unsigned short* wqkv; const unsigned short* wo;
  char* ws; unsigned short* out;
};

DEV float bf2f(u32 h) { return __builtin_bit_cast(float, h << 16); }
DEV float bflo(u32 w) { return __builtin_bit_cast(float, w << 16); }
DEV float bfhi(u32 w) { return __builtin_bit_cast(float, w & 0xffff0000u); }
DEV u32 pk2(float lo, float hi) { f32x2 f = {lo, hi}; return __builtin_bit_cast(u32, __builtin_convertvector(f, bf16x2)); }
DEV float rbf(float x) { return bflo(pk2(x, x)); }
DEV f32x4 mfma32(u32x4 a, u32x4 b, f32x4 c) {
  return __builtin_amdgcn_mfma_f32_16x16x32_bf16(__builtin_bit_cast(bf16x8, a), __builtin_bit_cast(bf16x8, b), c, 0, 0, 0);
}
DEV f32x4 mfma16(u32x2 a, u32x2 b, f32x4 c) {
  return __builtin_amdgcn_mfma_f32_16x16x16bf16_1k(__builtin_bit_cast(s16x4, a), __builtin_bit_cast(s16x4, b), c, 0, 0, 0);
}
DEV float dot2(u32 a, u32 b, float c) { return __builtin_amdgcn_fdot2_f32_bf16(__builtin_bit_cast(bf16x2, a), __builtin_bit_cast(bf16x2, b), c, false); }
typedef __amdgpu_buffer_rsrc_t rsrc_t;
DEV rsrc_t mk_rsrc(const void* p, u32 bytes) { return __builtin_amdgcn_make_buffer_rsrc((void*)p, (short)0, (int)bytes, 0x00020000); }
template <int AUX> DEV u32x4 ld16(rsrc_t r, u32 off) { return __builtin_amdgcn_raw_buffer_load_b128(r, off, 0, AUX); }
template <int AUX> DEV u32x2 ld8(rsrc_t r, u32 off) { return __builtin_amdgcn_raw_buffer_load_b64(r, off, 0, AUX); }
template <int AUX> DEV u32 ld4(rsrc_t r, u32 off) { return __builtin_amdgcn_raw_buffer_load_b32(r, off, 0, AUX); }
template <int AUX> DEV void st16(rsrc_t r, u32 off, u32x4 v) { __builtin_amdgcn_raw_buffer_store_b128(v, r, off, 0, AUX); }
template <int AUX> DEV void st8(rsrc_t r, u32 off, u32x2 v) { __builtin_amdgcn_raw_buffer_store_b64(v, r, off, 0, AUX); }
template <int AUX> DEV void st4(rsrc_t r, u32 off, u32 v) { __builtin_amdgcn_raw_buffer_store_b32(v, r, off, 0, AUX); }
DEV u32x4 lds_ld16(const char* s, u32 off) { return *(const LAS u32x4*)((const LAS char*)s + off); }
DEV u32x2 lds_ld8(const char* s, u32 off) { return *(const LAS u32x2*)((const LAS char*)s + off); }
DEV u32 lds_ld4(const char* s, u32 off) { return *(const LAS u32*)((const LAS char*)s + off); }
DEV void lds_st16(char* s, u32 off, u32x4 v) { *(LAS u32x4*)((LAS char*)s + off) = v; }
DEV void lds_st8(char* s, u32 off, u32x2 v) { *(LAS u32x2*)((LAS char*)s + off) = v; }
DEV void lds_st4(char* s, u32 off, u32 v) { *(LAS u32*)((LAS char*)s + off) = v; }
DEV u32x2 lds_tr(const char* s, u32 off) {
  return __builtin_bit_cast(u32x2, __builtin_amdgcn_ds_read_tr16_b64_v4i16((LAS s16x4*)((LAS char*)s + off)));
}
DEV float f_(u32 x) { return __builtin_bit_cast(float, x); }
DEV u32 u_(float x) { return __builtin_bit_cast(u32, x); }
DEV float xor16(float v) { return __shfl_xor(v, 16); }
DEV float xor32(float v) { return __shfl_xor(v, 32); }
DEV void bar() { asm volatile("s_waitcnt lgkmcnt(0)\n\ts_barrier" ::: "memory"); }
DEV int otid() { int t = threadIdx.x; asm volatile("" : "+v"(t)); return t; }
DEV void sfence() { __builtin_amdgcn_sched_barrier(0); }
constexpr u32 SENT = 0xffffffffu;
constexpr int SC1 = 16;
#define GC 8
#define GQ 0
#define GO 1
#define WC 0
#define WQ1 0
#define WO1 0
#define SLP 1
constexpr int SPIN_CAP = 1 << 14;
DEV bool isbad(u32x4 v) { return max(max(v[0], v[1]), max(v[2], v[3])) == SENT; }
DEV bool isbad(u32x2 v) { return max(v[0], v[1]) == SENT; }
DEV bool anyb(bool b) { return __builtin_amdgcn_ballot_w64(b) != 0ull; }
template <int N, class F> DEV void repoll16(rsrc_t r, F off, u32x4 (&v)[N]) {
  for (int spin = 0; spin < SPIN_CAP; ++spin) {
    u32 mask = 0;
#pragma unroll
    for (int i = 0; i < N; ++i) mask |= anyb(isbad(v[i])) ? (1u << i) : 0u;
    if (!mask) break;
    __builtin_amdgcn_s_sleep(SLP);
#pragma unroll
    for (int i = 0; i < N; ++i) if (mask & (1u << i)) v[i] = ld16<SC1>(r, off(i));
  }
}
template <int N, int G, bool ONEW, class F> DEV void poll16(rsrc_t r, F off, u32x4 (&v)[N]) {
  if (G >= 0) {
    if (!ONEW || threadIdx.x < 64) {
      v[G >= 0 ? G : 0] = ld16<SC1>(r, off(G));
      for (int spin = 0; spin < SPIN_CAP; ++spin) {
        if (!anyb(isbad(v[G >= 0 ? G : 0]))) break;
        __builtin_amdgcn_s_sleep(SLP);
        v[G >= 0 ? G : 0] = ld16<SC1>(r, off(G));
      }
    }
    if (ONEW) asm volatile("s_barrier" ::: "memory");
  }
#pragma unroll
  for (int i = 0; i < N; ++i) if (i != G || ONEW) v[i] = ld16<SC1>(r, off(i));
  repoll16<N>(r, off, v);
}
DEV int permk(int g, int j) { return j < 4 ? 4 * g + j : 16 + 4 * g + (j - 4); }
DEV void ln_stats(float sum, float sq, float& m, float& rinv) {
  float mean = sum * (1.0f / 512.0f);
  float var = fmaxf(sq * (1.0f / 512.0f) - mean * mean, 0.0f);
  m = rbf(mean);
  float den = rbf(__builtin_amdgcn_sqrtf(rbf(rbf(var) + 1e-5f)));
  rinv = __builtin_amdgcn_rcpf(den);
}
DEV f32x2 unpk(u32 w) { return f32x2{bflo(w), bfhi(w)}; }
DEV f32x2 rbf2(f32x2 v) { return unpk(pk2(v[0], v[1])); }
DEV f32x2 lo2(f32x4 v) { return f32x2{v[0], v[1]}; }
DEV f32x2 hi2(f32x4 v) { return f32x2{v[2], v[3]}; }
DEV u32 phi2(f32x2 a) {
  f32x2 t = rbf2(a), s = t * 1.4426950408889634f, p = t + 1.0f;
  f32x2 e = {__builtin_amdgcn_exp2f(s[0]), __builtin_amdgcn_exp2f(s[1])};
  f32x2 q = {t[0] > 0.0f ? p[0] : e[0], t[1] > 0.0f ? p[1] : e[1]};
  q = rbf2(q) + 1e-6f;
  return pk2(q[0], q[1]);
}
DEV float phi(float t) { float p = t > 0.0f ? t + 1.0f : __expf(t); return rbf(rbf(p) + 1e-6f); }
#define STAMP(i)
#define STAMP_WS(p)
#define STAMP_ARG
#define STAMP_PASS
constexpr u32 KA_ROW = 528;
constexpr u32 KA_TAB = 16 * KA_ROW;
constexpr u32 KA_G = 0, KA_E = 8 * KA_TAB;
constexpr u32 KA_KC = 16 * KA_TAB;
constexpr u32 KA_XT = KA_KC + 2 * 656 * 4;
constexpr u32 KA_LDS = KA_XT + 8 * 1040;
#define KST 16
struct cf { float r, i; };
DEV cf cmul(cf a, cf b) { cf c; c.r = a.r * b.r - a.i * b.i; c.i = a.r * b.i + a.i * b.r; return c; }
DEV cf cexp_pow(float are, float aim, int k) {
  float s = (float)(1 << k);
  float mag = __expf(are * s);
  double rev = (double)aim * (0.15915494309189535 * (double)(1 << k));
  float fr = (float)(rev - __builtin_floor(rev));
  cf c; c.r = mag * __builtin_amdgcn_cosf(fr); c.i = mag * __builtin_amdgcn_sinf(fr); return c;
}
DEV void phase_A(const Params& p, char* sm) {
  const int tid = threadIdx.x, b = blockIdx.x, lane = tid & 63, w = tid >> 6;
  rsrc_t rw = mk_rsrc(p.ws, O_END);
  const int rg = (b & 7) * 32 + (b >> 3);
  const int row = rg * 8 + w;
  u32x4 xv = *(const u32x4*)(p.u + (size_t)row * 256 + lane * 4);
  u32 wv[8]; u32 wdst;
  {
    int g = b * NT + tid, ln = g & 63, lq = ln >> 4;
    const unsigned short* src; int ld, ks;
    if (g < 98304) {
      int f = g >> 6, ty = f % 3, f2 = f / 3, ks8 = f2 & 7, kh = (f2 >> 3) & 1, hh = (f2 >> 4) & 3, cb = f2 >> 6;
      ks = kh * 8 + ks8; src = p.wqkv + ty * 512 + (4 * cb + hh) * 16 + (ln & 15); ld = 1536; wdst = O_WQ + (u32)g * 16;
    } else {
      int g2 = g - 98304, f = g2 >> 6; ks = f & 15; int ww = (f >> 4) & 7, cb4 = f >> 7;
      src = p.wo + 128 * cb4 + 16 * ww + (ln & 15); ld = 512; wdst = O_WO + (u32)g2 * 16;
    }
#pragma unroll
    for (int j = 0; j < 8; ++j) wv[j] = src[(size_t)(32 * ks + permk(lq, j)) * ld];
  }
  const int n = tid & 127, ch = (tid >> 7) & 1, sih = tid >> 8;
  const int d = 2 * b + ch;
  float lA = bf2f(p.logA[d * 128 + n]), Ai = bf2f(p.Aim[d * 128 + n]);
  float Cr = bf2f(p.Cre[d * 128 + n]), Ci = bf2f(p.Cim[d * 128 + n]);
  float dt = rbf(expf(bf2f(p.logdt[d])));
  {
    const u32 g = (u32)(b * NT + tid) * 16;
    const u32x4 sv = {SENT, SENT, SENT, SENT};
    st16<KST>(rw, O_X1 + g, sv);
    if (g < O_END - O_ZP) st16<KST>(rw, O_ZP + g, sv);
    if (g < 4 * L * 8) st16<KST>(rw, O_ST2 + g, sv);
  }
  {
    float s = 0.f, q = 0.f;
#pragma unroll
    for (int j = 0; j < 4; ++j) { s = dot2(xv[j], 0x3f803f80u, s); q = dot2(xv[j], xv[j], q); }
#pragma unroll
    for (int o = 1; o < 64; o <<= 1) { s += __shfl_xor(s, o); q += __shfl_xor(q, o); }
    if (lane == 0) { u32x2 v = {u_(s), u_(q)}; st8<KST>(rw, O_ST0 + row * 8, v); }
    lds_st16(sm, KA_XT + w * 1040 + lane * 16, xv);
  }
  { u32x4 v = {wv[0] | (wv[1] << 16), wv[2] | (wv[3] << 16), wv[4] | (wv[5] << 16), wv[6] | (wv[7] << 16)}; st16<KST>(rw, wdst, v); }
  float are = -rbf(expf(lA)) * dt, aim = Ai * dt;
  cf C; C.r = Cr * dt; C.i = Ci * dt;
#pragma unroll
  for (int t = 0; t < 2; ++t) {
    const int si = sih + 2 * t;
    const u32 tb = (u32)(ch * 4 + si) * KA_TAB + n * 4;
    cf v0 = cexp_pow(are, aim, si), v1 = cmul(v0, v0), v2 = cmul(v1, v1), v3 = cmul(v2, v2);
    cf e[8]; e[0].r = 1.f; e[0].i = 0.f; e[1] = v0; e[2] = v1; e[3] = cmul(v1, v0); e[4] = v2;
    e[5] = cmul(v2, v0); e[6] = cmul(v2, v1); e[7] = cmul(v2, e[3]);
#pragma unroll
    for (int j = 0; j < 8; ++j) {
      lds_st4(sm, KA_E + tb + j * KA_ROW, pk2(e[j].r, e[j].i));
      cf f = cmul(e[j], v3);
      lds_st4(sm, KA_E + tb + (j + 8) * KA_ROW, pk2(f.r, f.i));
    }
    cf g0 = cexp_pow(are, aim, 4 + si), g1 = cmul(g0, g0), g2 = cmul(g1, g1), g3 = cmul(g2, g2);
    cf h[8]; h[0] = (si == 0) ? C : cmul(C, g3);
    h[1] = cmul(h[0], g0); h[2] = cmul(h[0], g1); h[3] = cmul(h[1], g1);
#pragma unroll
    for (int j = 0; j < 4; ++j) h[4 + j] = cmul(h[j], g2);
#pragma unroll
    for (int j = 0; j < 8; ++j) {
      lds_st4(sm, KA_G + tb + j * KA_ROW, pk2(h[j].r, -h[j].i));
      cf f = cmul(h[j], g3);
      lds_st4(sm, KA_G + tb + (j + 8) * KA_ROW, pk2(f.r, -f.i));
    }
  }
  __syncthreads();
  {
    u32 v[8];
#pragma unroll
    for (int r = 0; r < 8; ++r) v[r] = *(const LAS unsigned short*)((LAS char*)sm + KA_XT + r * 1040 + tid * 2);
    u32x4 o = {v[0] | (v[1] << 16), v[2] | (v[3] << 16), v[4] | (v[5] << 16), v[6] | (v[7] << 16)};
    st16<KST>(rw, O_X0 + (((rg >> 2) * 512 + tid) * 32 + (rg & 3) * 8) * 2, o);
  }
  {
    const int wc = w >> 2, si = w & 3, lq = lane >> 4, r16 = lane & 15;
    const u32 base = (u32)(wc * 4 + si) * KA_TAB + r16 * KA_ROW + lq * 16;
    f32x4 acc = {0.f, 0.f, 0.f, 0.f};
#pragma unroll
    for (int ks = 0; ks < 8; ++ks) acc = mfma32(lds_ld16(sm, KA_G + base + ks * 64), lds_ld16(sm, KA_E + base + ks * 64), acc);
#pragma unroll
    for (int r = 0; r < 4; ++r) {
      int i = 4 * lq + r;
      int idx = si == 0 ? 16 * i + r16 : 256 + (si - 1) * 128 + 16 * i + r16;
      bool ok = si == 0 || i < 8 || (si == 3 && i == 8 && r16 == 0);
      if (ok) lds_st4(sm, KA_KC + (wc * 656 + idx) * 4, u_(2.0f * acc[r]));
    }
  }
  __syncthreads();
  {
    const int c2 = tid >> 8, m = tid & 255;
    u32 o[4];
#pragma unroll
    for (int j = 0; j < 4; ++j) {
      float kv[2];
#pragma unroll
      for (int h = 0; h < 2; ++h) {
        int l = 2047 - (8 * m + 2 * j + h);
        int e = l < 256 ? 0 : (l < 512 ? 1 : (l < 1024 ? 2 : 3));
        int mm = (l + ((1 << e) >> 1) - 1 + ((l >> e) & 1)) >> e;
        int idx = e == 0 ? l : 256 + (e - 1) * 128 + (mm - 128);
        kv[h] = f_(lds_ld4(sm, KA_KC + (c2 * 656 + idx) * 4));
      }
      o[j] = pk2(kv[0], kv[1]);
    }
    u32x4 ov = {o[0], o[1], o[2], o[3]};
    const u32 kb = O_KR + (u32)(2 * b + c2) * KR_STR;
    st16<KST>(rw, kb + m * 16, ov);
    if (m < 2) { u32x4 z = {0u, 0u, 0u, 0u}; st16<KST>(rw, kb + 4096 + m * 16, z); }
  }
}
#define PF 0
constexpr u32 P_G = 0, P_GSTR = 2064 * 16;
constexpr u32 DB = 2 * P_GSTR;
constexpr u32 P_CS = 158720;
constexpr u32 P_LDS = P_CS + 8 * 3 * 16 * 4;
constexpr u32 C_U = DB, C_ST = C_U + 2 * 4608, C_RED = C_ST + L * 8;
constexpr u32 Q_A = DB, Q_EX = DB, Q_SP = DB + 65536, Q_MR = Q_SP + 1024;
constexpr u32 O_OF = DB, O_T = DB + 32768, O_PS = O_T + 128 * 144;
constexpr u32 R_B = DB;
static_assert(C_RED + 65536 <= P_CS && Q_MR + 8 * 512 <= P_CS && P_LDS <= 163840, "LDS map");

DEV void fill16(rsrc_t rw, u32 off) { st16<SC1>(rw, off, u32x4{SENT, SENT, SENT, SENT}); }
DEV void conv_load_K(rsrc_t rw, u32x4 (&a)[4], u32 (&e)[4], int d0) {
  const int tid = otid(), ch = tid >> 8, y0 = tid & 255;
  const u32 kb = O_KR + (u32)(d0 + ch) * KR_STR + y0 * 4;
#pragma unroll
  for (int i = 0; i < 4; ++i) { a[i] = ld16<0>(rw, kb + i * 1024); e[i] = ld4<0>(rw, kb + i * 1024 + 16); }
}
DEV void conv_build_G(char* sm, const u32x4 (&a)[4], const u32 (&e)[4]) {
  const int tid = otid(), ch = tid >> 8, y0 = tid & 255;
#pragma unroll
  for (int i = 0; i < 4; ++i) {
    const u32 gb = P_G + ch * P_GSTR + (y0 + 256 * i) * 32;
    lds_st16(sm, gb, a[i]);
    u32x4 o = {__builtin_amdgcn_alignbit(a[i][1], a[i][0], 16), __builtin_amdgcn_alignbit(a[i][2], a[i][1], 16),
               __builtin_amdgcn_alignbit(a[i][3], a[i][2], 16), __builtin_amdgcn_alignbit(e[i], a[i][3], 16)};
    lds_st16(sm, gb + 16, o);
  }
  if (y0 < 16) lds_st16(sm, P_G + ch * P_GSTR + (2048 + y0) * 16, u32x4{0u, 0u, 0u, 0u});
}
template <int NP, bool POLL>
DEV void phase_C(rsrc_t rw, char* sm, u32x4 (&W)[8][3], int cb, int d0, float dsk, u32 o_in, u32 o_st, u32 o_out, int sb STAMP_ARG) {
  const int tid = otid(), lane = tid & 63, w = tid >> 6, ch = tid >> 8, m = tid & 255;
  const u32 xoff = (((m >> 2) * 512 + d0 + ch) * 32 + (m & 3) * 8) * 2;
  u32x4 xv;
  {
    u32x4 sv[2 * NP + 1];
    auto off = [&](int i) -> u32 { return i == 2 * NP ? o_in + xoff : o_st + (i >> 1) * L * 8 + tid * 32 + (i & 1) * 16; };
    u32x4 ka[4]; u32 ke[4];
    if (POLL) poll16<2 * NP + 1, (POLL ? GC : -1), WC>(rw, off, sv);
    else {
      conv_load_K(rw, ka, ke, d0);
#pragma unroll
      for (int i = 0; i < 2 * NP + 1; ++i) sv[i] = ld16<0>(rw, off(i));
      const u32 fg = (u32)(blockIdx.x * NT + tid) * 16;
      fill16(rw, O_PQ + fg); fill16(rw, O_KVP + fg);
    }
    xv = sv[2 * NP];
    if (!POLL) conv_build_G(sm, ka, ke);
    float mr[8];
#pragma unroll
    for (int r = 0; r < 4; ++r) {
      float s = 0.f, q2 = 0.f;
#pragma unroll
      for (int q = 0; q < NP; ++q) { s += f_(sv[2 * q + (r >> 1)][(r & 1) * 2]); q2 += f_(sv[2 * q + (r >> 1)][(r & 1) * 2 + 1]); }
      ln_stats(s, q2, mr[2 * r], mr[2 * r + 1]);
    }
    lds_st16(sm, C_ST + tid * 32, u32x4{u_(mr[0]), u_(mr[2]), u_(mr[1]), u_(mr[3])});
    lds_st16(sm, C_ST + tid * 32 + 16, u32x4{u_(mr[4]), u_(mr[6]), u_(mr[5]), u_(mr[7])});
    if (tid < 64) lds_st16(sm, C_U + (tid >> 5) * 4608 + ((tid >> 4) & 1) * 2304 + (tid & 15) * 16, u32x4{0u, 0u, 0u, 0u});
  }
  asm volatile("s_waitcnt vmcnt(0)" ::: "memory");
  bar();
  STAMP(sb + 1);
  u32x4 uv;
#pragma unroll
  for (int j = 0; j < 4; ++j) {
    u32x4 s = lds_ld16(sm, C_ST + (8 * m + 2 * j) * 8);
    f32x2 t = rbf2(unpk(xv[j]) - f32x2{f_(s[0]), f_(s[1])}) * f32x2{f_(s[2]), f_(s[3])};
    uv[j] = pk2(t[0], t[1]);
  }
  lds_st16(sm, C_U + ch * 4608 + (m & 1) * 2304 + (16 + (m >> 1)) * 16, uv);
  bar();
  {
    const int wc = w >> 2, w4 = w & 3, lq = lane >> 4, r16 = lane & 15;
    const int j0 = (lq & 1) * 16 + (lq >> 1) * 8, rho = 2 * w4;
    const u32 ub = C_U + wc * 4608 + (lq >> 1) * 2304 + (15 - 2 * rho + r16 + (lq & 1)) * 16;
    const u32 gb = P_G + wc * P_GSTR + (2031 - 32 * (rho + 1) - 256 * 7 - r16 + j0) * 16;
    f32x4 acc[8];
#pragma unroll
    for (int c = 0; c < 8; ++c) acc[c] = f32x4{0.f, 0.f, 0.f, 0.f};
    u32x4 B0[8], A0[8], B1[8], A1[8];
#pragma unroll
    for (int k = 0; k < 8; ++k) { A0[k] = lds_ld16(sm, gb + 512 + (7 - k) * 4096); B0[k] = lds_ld16(sm, ub + k * 256); }
    sfence();
    const u32 wb = O_WQ + (u32)(cb * 8 + w) * 24576 + lane * 16;
#pragma unroll
    for (int g = 0; g < 8; ++g) {
      A1[g] = lds_ld16(sm, gb + (7 - g) * 4096); B1[g] = lds_ld16(sm, ub - 32 + g * 256);
#pragma unroll
      for (int i = 2 * g; i < 2 * g + 2; ++i) W[i / 3][i % 3] = ld16<0>(rw, wb + i * 1024);
      sfence();
#pragma unroll
      for (int c = 7; c >= g; --c) acc[c] = mfma32(A0[g], B0[c - g], acc[c]);
      sfence();
    }
#pragma unroll
    for (int g = 0; g < 8; ++g) {
      W[(16 + g) / 3][(16 + g) % 3] = ld16<0>(rw, wb + (16 + g) * 1024);
      sfence();
#pragma unroll
      for (int c = g; c < 8; ++c) acc[c] = mfma32(A1[g], B1[c - g], acc[c]);
      sfence();
    }
#pragma unroll
    for (int c = 0; c < 8; ++c) lds_st16(sm, C_RED + ((wc * 4 + w4) * 8 + c) * 1024 + lane * 16, __builtin_bit_cast(u32x4, acc[c]));
  }
  bar();
  STAMP(sb + 2);
  {
    const int c = m >> 5, a = (m & 31) >> 1, lq0 = (m & 1) * 2;
    f32x4 y0 = {0.f, 0.f, 0.f, 0.f}, y1 = y0;
#pragma unroll
    for (int q = 0; q < 4; ++q) {
      const u32 o = C_RED + ((ch * 4 + q) * 8 + c) * 1024 + (lq0 * 16 + a) * 16;
      y0 += __builtin_bit_cast(f32x4, lds_ld16(sm, o)); y1 += __builtin_bit_cast(f32x4, lds_ld16(sm, o + 256));
    }
    u32x4 ov;
#pragma unroll
    for (int j = 0; j < 4; ++j) {
      f32x2 y = j == 0 ? lo2(y0) : j == 1 ? hi2(y0) : j == 2 ? lo2(y1) : hi2(y1);
      f32x2 r = unpk(xv[j]) + rbf2(rbf2(y) + rbf2(unpk(uv[j]) * dsk));
      ov[j] = pk2(r[0], r[1]);
    }
    st16<SC1>(rw, o_out + xoff, ov);
  }
  bar();
}

DEV void q_colsum(char* sm, const u32x4 (&W)[8][3]) {
  const int tid = otid(), lane = tid & 63, w = tid >> 6;
#pragma unroll
  for (int ty = 0; ty < 3; ++ty) {
    float s = 0.f;
#pragma unroll
    for (int ks = 0; ks < 8; ++ks)
#pragma unroll
      for (int j = 0; j < 4; ++j) s = dot2(W[ks][ty][j], 0x3f803f80u, s);
    s += xor16(s); s += xor32(s);
    if (lane < 16) lds_st4(sm, P_CS + ((w * 3 + ty) * 16 + lane) * 4, u_(s));
  }
}
template <bool FIRST>
DEV void phase_Q(rsrc_t rw, char* sm, const u32x4 (&W)[8][3], u32x4 (&Wo)[16], int rb, int cb, int cb4, u32 o_x, u32 o_pq, u32 o_kvp, u32 o_zp,
                 int sb STAMP_ARG) {
  const int tid = otid(), lane = tid & 63, w = tid >> 6, hh = w >> 1, kh = w & 1, lq = lane >> 4, r16 = lane & 15;
  {
    u32x4 av[8];
    poll16<8, GQ, WQ1>(rw, [&](int i) -> u32 { return o_x + (u32)rb * 65536 + (tid + i * 512) * 16; }, av);
    const int chn = tid >> 2, pc = tid & 3, f = (chn >> 1) & 3;
#pragma unroll
    for (int i = 0; i < 8; ++i)
      lds_st16(sm, Q_A + (chn + 128 * (i & 3)) * 128 + ((((i >> 2) * 2 + (pc >> 1)) ^ f) * 32) + (pc & 1) * 16, av[i]);
  }
  bar();
  STAMP(sb + 1);
  f32x4 acc[4][3];
#pragma unroll
  for (int rt = 0; rt < 4; ++rt)
#pragma unroll
    for (int ty = 0; ty < 3; ++ty) acc[rt][ty] = f32x4{0.f, 0.f, 0.f, 0.f};
  const int chl = 4 * lq + (r16 >> 2), f = (chl >> 1) & 3;
  u32 ab[4];
#pragma unroll
  for (int rt = 0; rt < 4; ++rt) ab[rt] = Q_A + (kh * 256 + chl) * 128 + ((rt ^ f) * 32) + (r16 & 3) * 8;
  const u32 abs_ = Q_A + (kh * 256 + chl) * 128 + ((hh ^ f) * 32) + (r16 & 3) * 8;
  float ssum = 0.f, ssq = 0.f;
  const u32 wob = O_WO + (u32)(cb4 * 8 + w) * 16384 + lane * 16;
  {
    u32x2 xa[2][4][2], xs[2][2];
#pragma unroll
    for (int rt = 0; rt < 4; ++rt) { xa[0][rt][0] = lds_tr(sm, ab[rt]); xa[0][rt][1] = lds_tr(sm, ab[rt] + 2048); }
    xs[0][0] = lds_tr(sm, abs_); xs[0][1] = lds_tr(sm, abs_ + 2048);
#pragma unroll
    for (int ks = 0; ks < 8; ++ks) {
      const int cu = ks & 1, nx = cu ^ 1;
      if (ks < 7) {
#pragma unroll
        for (int rt = 0; rt < 4; ++rt) { xa[nx][rt][0] = lds_tr(sm, ab[rt] + (ks + 1) * 4096); xa[nx][rt][1] = lds_tr(sm, ab[rt] + (ks + 1) * 4096 + 2048); }
        xs[nx][0] = lds_tr(sm, abs_ + (ks + 1) * 4096); xs[nx][1] = lds_tr(sm, abs_ + (ks + 1) * 4096 + 2048);
      }
      Wo[2 * ks] = ld16<0>(rw, wob + 2 * ks * 1024); Wo[2 * ks + 1] = ld16<0>(rw, wob + (2 * ks + 1) * 1024);
      if (FIRST && ks == 1) fill16(rw, O_X2 + (u32)(blockIdx.x * NT + tid) * 16);
      if (FIRST && ks == 4) fill16(rw, O_X3 + (u32)(blockIdx.x * NT + tid) * 16);
      sfence();
#pragma unroll
      for (int rt = 0; rt < 4; ++rt) {
        u32x4 X = {xa[cu][rt][0][0], xa[cu][rt][0][1], xa[cu][rt][1][0], xa[cu][rt][1][1]};
        acc[rt][0] = mfma32(W[ks][0], X, acc[rt][0]);
        acc[rt][1] = mfma32(X, W[ks][1], acc[rt][1]);
        acc[rt][2] = mfma32(X, W[ks][2], acc[rt][2]);
      }
#pragma unroll
      for (int j = 0; j < 2; ++j)
#pragma unroll
        for (int e = 0; e < 2; ++e) { ssum = dot2(xs[cu][j][e], 0x3f803f80u, ssum); ssq = dot2(xs[cu][j][e], xs[cu][j][e], ssq); }
      sfence();
    }
  }
  ssum += xor16(ssum); ssum += xor32(ssum); ssq += xor16(ssq); ssq += xor32(ssq);
  if (lane < 16) lds_st8(sm, Q_SP + (kh * 64 + hh * 16 + lane) * 8, u32x2{u_(ssum), u_(ssq)});
  bar();
  const u32 ex = Q_EX + hh * 12288 + lane * 16;
  if (kh == 0) {
#pragma unroll
    for (int rt = 0; rt < 4; ++rt) lds_st16(sm, ex + rt * 1024, __builtin_bit_cast(u32x4, acc[rt][0]));
  } else {
#pragma unroll
    for (int rt = 0; rt < 4; ++rt) {
      lds_st16(sm, ex + (4 + rt * 2) * 1024, __builtin_bit_cast(u32x4, acc[rt][1]));
      lds_st16(sm, ex + (5 + rt * 2) * 1024, __builtin_bit_cast(u32x4, acc[rt][2]));
    }
  }
  {
    u32x2 p0 = lds_ld8(sm, Q_SP + lane * 8), p1 = lds_ld8(sm, Q_SP + (64 + lane) * 8);
    float mean, rinv; ln_stats(f_(p0[0]) + f_(p1[0]), f_(p0[1]) + f_(p1[1]), mean, rinv);
    lds_st4(sm, Q_MR + w * 512 + lane * 4, u_(mean)); lds_st4(sm, Q_MR + w * 512 + 256 + lane * 4, u_(rinv));
  }
  bar();
  STAMP(sb + 2);
  const int h = cb * 4 + hh;
  const u32 mrb = Q_MR + w * 512, csb = P_CS + hh * 2 * 3 * 16 * 4;
  if (kh == 1) {
    const f32x4 cs = __builtin_bit_cast(f32x4, lds_ld16(sm, csb + 16 * lq)) + __builtin_bit_cast(f32x4, lds_ld16(sm, csb + 192 + 16 * lq));
#pragma unroll
    for (int rp = 0; rp < 2; ++rp) {
      u32x4 ov;
#pragma unroll
      for (int t = 0; t < 2; ++t) {
        const int rt = rp * 2 + t;
        f32x4 a = acc[rt][0] + __builtin_bit_cast(f32x4, lds_ld16(sm, ex + rt * 1024));
        const float mean = f_(lds_ld4(sm, mrb + (rt * 16 + r16) * 4)), rinv = f_(lds_ld4(sm, mrb + 256 + (rt * 16 + r16) * 4));
        ov[2 * t] = phi2((lo2(a) - lo2(cs) * mean) * rinv); ov[2 * t + 1] = phi2((hi2(a) - hi2(cs) * mean) * rinv);
      }
      st16<SC1>(rw, o_pq + (((rb * 2 + rp) * 32 + h) * 64 + lane) * 16, ov);
    }
  } else {
    const float csk = f_(lds_ld4(sm, csb + 64 + r16 * 4)) + f_(lds_ld4(sm, csb + 192 + 64 + r16 * 4));
    const float csv = f_(lds_ld4(sm, csb + 128 + r16 * 4)) + f_(lds_ld4(sm, csb + 192 + 128 + r16 * 4));
    f32x4 kv = {0.f, 0.f, 0.f, 0.f}, zz = kv;
#pragma unroll
    for (int rp = 0; rp < 2; ++rp) {
      u32x4 pk, vv;
#pragma unroll
      for (int t = 0; t < 2; ++t) {
        const int rt = rp * 2 + t;
        f32x4 ak = acc[rt][1] + __builtin_bit_cast(f32x4, lds_ld16(sm, ex + (4 + rt * 2) * 1024));
        f32x4 av = acc[rt][2] + __builtin_bit_cast(f32x4, lds_ld16(sm, ex + (5 + rt * 2) * 1024));
        const f32x4 mean = __builtin_bit_cast(f32x4, lds_ld16(sm, mrb + (rt * 16 + 4 * lq) * 4));
        const f32x4 rinv = __builtin_bit_cast(f32x4, lds_ld16(sm, mrb + 256 + (rt * 16 + 4 * lq) * 4));
        pk[2 * t] = phi2((lo2(ak) - lo2(mean) * csk) * lo2(rinv)); pk[2 * t + 1] = phi2((hi2(ak) - hi2(mean) * csk) * hi2(rinv));
        f32x2 v0 = (lo2(av) - lo2(mean) * csv) * lo2(rinv), v1 = (hi2(av) - hi2(mean) * csv) * hi2(rinv);
        vv[2 * t] = pk2(v0[0], v0[1]); vv[2 * t + 1] = pk2(v1[0], v1[1]);
      }
      kv = mfma32(pk, vv, kv);
      zz = mfma32(pk, u32x4{0x3f803f80u, 0x3f803f80u, 0x3f803f80u, 0x3f803f80u}, zz);
    }
    st16<SC1>(rw, o_kvp + ((rb * 32 + h) * 64 + lane) * 16, __builtin_bit_cast(u32x4, kv));
    if (r16 == 0) st16<SC1>(rw, o_zp + (rb * 32 + h) * 64 + lq * 16, __builtin_bit_cast(u32x4, zz));
  }
  bar();
}

DEV void phase_R(rsrc_t rw, char* sm, int b, u32 o_kvp, u32 o_zp, u32 o_kvt, u32 o_zt) {
  const int tid = otid(), h = b >> 3, j = b & 7, rb = tid >> 4, q = tid & 15;
  u32x4 v[1];
  const u32 off = q < 8 ? o_kvp + ((rb * 32 + h) * 64 + 8 * j + q) * 16 : o_zp + (rb * 32 + h) * 64 + (j >> 1) * 16;
  poll16<1, -1, false>(rw, [&](int) -> u32 { return off; }, v);
  asm volatile("s_waitcnt vmcnt(0)" ::: "memory");
  if (q < 8) lds_st16(sm, R_B + (rb * 40 + q * 4) * 4, v[0]);
  else if (q == 8) lds_st8(sm, R_B + (rb * 40 + 32) * 4, (j & 1) ? u32x2{v[0][2], v[0][3]} : u32x2{v[0][0], v[0][1]});
  bar();
  if (tid < 64) {
    const int e = tid < 34 ? tid : 0;
    float s = 0.f;
#pragma unroll
    for (int r = 0; r < 32; ++r) s += f_(lds_ld4(sm, R_B + (r * 40 + e) * 4));
    float o = __shfl_down(s, 1), o2 = __shfl_down(s, 2), o3 = __shfl_down(s, 3);
    if (tid < 32 && (tid & 3) == 0) st8<SC1>(rw, o_kvt + (h * 64 + 8 * j + (tid >> 2)) * 8, u32x2{pk2(s, o), pk2(o2, o3)});
    if (tid == 32) st4<SC1>(rw, o_zt + h * 32 + j * 4, pk2(s, o));
  }
  bar();
}

DEV void o_prefetch(rsrc_t rw, u32x4 (&v)[5], int rb, int cb4, u32 o_pq, u32 o_xin) {
  const int tid = otid(), lane = tid & 63, w = tid >> 6;
#pragma unroll
  for (int i = 0; i < 4; ++i) v[i] = ld16<SC1>(rw, o_pq + ((rb * 32 + 4 * w + i) * 64 + lane) * 16);
  v[4] = ld16<SC1>(rw, o_xin + ((rb * 512 + cb4 * 128 + (tid >> 2)) * 32 + (tid & 3) * 8) * 2);
}
template <bool FINAL>
DEV void phase_O(unsigned short* out, rsrc_t rw, char* sm, const u32x4 (&Wo)[16], u32x4 (&v)[5], int rb, int cb4, u32 o_pq, u32 o_kvt, u32 o_zt,
                 u32 o_xin, u32 o_xout, u32 o_st, int sb STAMP_ARG) {
  const int tid = otid(), lane = tid & 63, w = tid >> 6, lq = lane >> 4, r16 = lane & 15;
  const int ecol = tid >> 2, epc = tid & 3;
  const u32 xoff = ((rb * 512 + cb4 * 128 + ecol) * 32 + epc * 8) * 2;
  u32x4 xin;
  {
    u32x2 kv[4], zt[4];
    if (GO) {
      if (!WO1 || tid < 64) {
        kv[3] = ld8<SC1>(rw, o_kvt + ((4 * w + 3) * 64 + lane) * 8);
        for (int spin = 0; spin < SPIN_CAP; ++spin) {
          if (!anyb(isbad(kv[3]))) break;
          __builtin_amdgcn_s_sleep(SLP);
          kv[3] = ld8<SC1>(rw, o_kvt + ((4 * w + 3) * 64 + lane) * 8);
        }
      }
      if (WO1) asm volatile("s_barrier" ::: "memory");
    }
#pragma unroll
    for (int i = 0; i < 4; ++i) { if (!GO || WO1 || i < 3) kv[i] = ld8<SC1>(rw, o_kvt + ((4 * w + i) * 64 + lane) * 8); zt[i] = ld8<SC1>(rw, o_zt + (4 * w + i) * 32 + lq * 8); }
    repoll16<5>(rw, [&](int i) -> u32 { return i < 4 ? o_pq + ((rb * 32 + 4 * w + i) * 64 + lane) * 16 : o_xin + xoff; }, v);
    xin = v[4];
    for (int spin = 0; spin < SPIN_CAP; ++spin) {
      bool bad = false;
#pragma unroll
      for (int i = 0; i < 4; ++i) bad = bad || isbad(kv[i]) || isbad(zt[i]);
      if (!anyb(bad)) break;
      __builtin_amdgcn_s_sleep(SLP);
#pragma unroll
      for (int i = 0; i < 4; ++i) { kv[i] = ld8<SC1>(rw, o_kvt + ((4 * w + i) * 64 + lane) * 8); zt[i] = ld8<SC1>(rw, o_zt + (4 * w + i) * 32 + lq * 8); }
    }
    STAMP(sb + 1);
#pragma unroll
    for (int hp = 0; hp < 2; ++hp)
#pragma unroll
      for (int rt = 0; rt < 2; ++rt) {
        u32x4 of;
#pragma unroll
        for (int t = 0; t < 2; ++t) {
          const int i = hp * 2 + t;
          u32x2 B = {v[i][2 * rt], v[i][2 * rt + 1]};
          f32x4 z4 = {0.f, 0.f, 0.f, 0.f};
          f32x4 num = mfma16(kv[i], B, z4), den = mfma16(zt[i], B, z4);
          const float dr = __builtin_amdgcn_rcpf(rbf(rbf(den[0]) + 1e-6f));
          f32x2 o0 = rbf2(lo2(num)) * dr, o1 = rbf2(hi2(num)) * dr;
          of[2 * t] = pk2(o0[0], o0[1]); of[2 * t + 1] = pk2(o1[0], o1[1]);
        }
        lds_st16(sm, O_OF + ((rt * 16 + 2 * w + hp) * 64 + lane) * 16, of);
      }
  }
  bar();
  {
    f32x4 a0 = {0.f, 0.f, 0.f, 0.f}, a1 = a0;
    u32x4 o0[16], o1[16];
#pragma unroll
    for (int ks = 0; ks < 4; ++ks) { o0[ks] = lds_ld16(sm, O_OF + (ks * 64 + lane) * 16); o1[ks] = lds_ld16(sm, O_OF + ((16 + ks) * 64 + lane) * 16); }
#pragma unroll
    for (int ks = 0; ks < 16; ++ks) {
      if (!FINAL && ks == 2) fill16(rw, O_PQ + XSZ + (u32)(blockIdx.x * NT + tid) * 16);
      if (ks + 4 < 16) { o0[ks + 4] = lds_ld16(sm, O_OF + ((ks + 4) * 64 + lane) * 16); o1[ks + 4] = lds_ld16(sm, O_OF + ((20 + ks) * 64 + lane) * 16); }
      sfence();
      a0 = mfma32(o0[ks], Wo[ks], a0);
      a1 = mfma32(o1[ks], Wo[ks], a1);
      sfence();
    }
    lds_st16(sm, O_T + (16 * w + r16) * 144 + (4 * lq) * 4, __builtin_bit_cast(u32x4, a0));
    lds_st16(sm, O_T + (16 * w + r16) * 144 + (16 + 4 * lq) * 4, __builtin_bit_cast(u32x4, a1));
  }
  bar();
  STAMP(sb + 2);
  f32x4 t0 = __builtin_bit_cast(f32x4, lds_ld16(sm, O_T + ecol * 144 + epc * 32)), t1 = __builtin_bit_cast(f32x4, lds_ld16(sm, O_T + ecol * 144 + epc * 32 + 16));
  u32x4 ov;
#pragma unroll
  for (int j = 0; j < 4; ++j) {
    f32x2 r = unpk(xin[j]) + rbf2(j == 0 ? lo2(t0) : j == 1 ? hi2(t0) : j == 2 ? lo2(t1) : hi2(t1));
    ov[j] = pk2(r[0], r[1]);
  }
  if (!FINAL) {
    st16<SC1>(rw, o_xout + xoff, ov);
#pragma unroll
    for (int j = 0; j < 8; ++j)
      *(LAS unsigned short*)((LAS char*)sm + O_OF + (epc * 8 + j) * 272 + ecol * 2) = (unsigned short)((j & 1) ? (ov[j >> 1] >> 16) : (ov[j >> 1] & 0xffffu));
    bar();
    const int r = tid >> 4, pc = tid & 15;
    u32x4 v = lds_ld16(sm, O_OF + r * 272 + pc * 16);
    float s = 0.f, q = 0.f;
#pragma unroll
    for (int j = 0; j < 4; ++j) { s = dot2(v[j], 0x3f803f80u, s); q = dot2(v[j], v[j], q); }
#pragma unroll
    for (int o = 1; o < 16; o <<= 1) { s += __shfl_xor(s, o); q += __shfl_xor(q, o); }
    float s1 = __shfl_down(s, 16), q1 = __shfl_down(q, 16);
    if ((lane & 31) == 0) st16<SC1>(rw, o_st + (cb4 * L + rb * 32 + r) * 8, u32x4{u_(s), u_(q), u_(s1), u_(q1)});
    bar();
  } else {
#pragma unroll
    for (int j = 0; j < 8; ++j)
      *(LAS unsigned short*)((LAS char*)sm + O_OF + (epc * 8 + j) * 272 + ecol * 2) = (unsigned short)((j & 1) ? (ov[j >> 1] >> 16) : (ov[j >> 1] & 0xffffu));
    bar();
    const int r = tid >> 4, pc = tid & 15;
    *(u32x4*)(out + (size_t)(rb * 32 + r) * 512 + cb4 * 128 + pc * 8) = lds_ld16(sm, O_OF + r * 272 + pc * 16);
  }
}

extern __shared__ __attribute__((aligned(16))) char smem[];
__global__ void __launch_bounds__(512, 2) kA(Params p) { STAMP_WS(p); STAMP(0); phase_A(p, smem); STAMP(1); }
__global__ void __launch_bounds__(512, 2) kB(Params p) {
  STAMP_WS(p); STAMP(2);
  rsrc_t rw = mk_rsrc(p.ws, O_END);
  const int b = blockIdx.x, tid = otid();
  const int d0 = 2 * ((b & 7) * 32 + (b >> 3));
  const int rbq = (b & 7) * 4 + ((b >> 3) & 3), cb = b >> 5;
  const int rbo = (b & 7) * 8 + ((b >> 3) & 7), cb4 = b >> 6;
  const float dsk = bf2f(p.Dskip[d0 + (tid >> 8)]);
  u32x4 W[8][3], Wo[16], ov5[5];
  phase_C<1, false>(rw, smem, W, cb, d0, dsk, O_X0, O_ST0, O_X1, 3 STAMP_PASS);
  q_colsum(smem, W);
  STAMP(6);
  phase_Q<true>(rw, smem, W, Wo, rbq, cb, cb4, O_X1, O_PQ, O_KVP, O_ZP, 6 STAMP_PASS);
  STAMP(9);
  if (PF) o_prefetch(rw, ov5, rbo, cb4, O_PQ, O_X1);
  phase_R(rw, smem, b, O_KVP, O_ZP, O_KVT, O_ZT);
  if (!PF) o_prefetch(rw, ov5, rbo, cb4, O_PQ, O_X1);
  STAMP(10);
  phase_O<false>(p.out, rw, smem, Wo, ov5, rbo, cb4, O_PQ, O_KVT, O_ZT, O_X1, O_X2, O_ST2, 10 STAMP_PASS);
  STAMP(13);
  phase_C<4, true>(rw, smem, W, cb, d0, dsk, O_X2, O_ST2, O_X3, 13 STAMP_PASS);
  STAMP(16);
  phase_Q<false>(rw, smem, W, Wo, rbq, cb, cb4, O_X3, O_PQ + XSZ, O_KVP + KVP_SZ, O_ZP + ZP_SZ, 16 STAMP_PASS);
  STAMP(19);
  if (PF) o_prefetch(rw, ov5, rbo, cb4, O_PQ + XSZ, O_X3);
  phase_R(rw, smem, b, O_KVP + KVP_SZ, O_ZP + ZP_SZ, O_KVT + KVT_SZ, O_ZT + ZT_SZ);
  if (!PF) o_prefetch(rw, ov5, rbo, cb4, O_PQ + XSZ, O_X3);
  STAMP(20);
  phase_O<true>(p.out, rw, smem, Wo, ov5, rbo, cb4, O_PQ + XSZ, O_KVT + KVT_SZ, O_ZT + ZT_SZ, O_X3, 0u, 0u, 20 STAMP_PASS);
  STAMP(23);
}
static Params make_params(void* const* d_in, void* d_out, void* d_ws) {
  Params p;
  p.u = (const u32*)d_in[0]; p.logA = (const unsigned short*)d_in[1]; p.Aim = (const unsigned short*)d_in[2];
  p.Cre = (const unsigned short*)d_in[3]; p.Cim = (const unsigned short*)d_in[4]; p.logdt = (const unsigned short*)d_in[5];
  p.Dskip = (const unsigned short*)d_in[6]; p.wqkv = (const unsigned short*)d_in[8]; p.wo = (const unsigned short*)d_in[9];
  p.ws = (char*)d_ws; p.out = (unsigned short*)d_out;
  return p;
}
extern "C" void kernel_launch(void* const* d_in, const int* in_sizes, int n_in, void* d_out, int out_size, void* d_ws, size_t ws_size,
                              hipStream_t stream) {
  Params p = make_params(d_in, d_out, d_ws);
  (void)hipFuncSetAttribute((const void*)kA, hipFuncAttributeMaxDynamicSharedMemorySize, KA_LDS);
  (void)hipFuncSetAttribute((const void*)kB, hipFuncAttributeMaxDynamicSharedMemorySize, P_LDS);
  kA<<<NB, NT, KA_LDS, stream>>>(p);
  kB<<<NB, NT, P_LDS, stream>>>(p);
}
```

```cpp
#include <hip/hip_runtime.h>
#include <hip/hip_bf16.h>
#include <stdint.h>

typedef unsigned u32;
typedef unsigned u32x4 __attribute__((ext_vector_type(4)));
typedef unsigned u32x2 __attribute__((ext_vector_type(2)));
typedef float f32x4 __attribute__((ext_vector_type(4)));
typedef float f32x2 __attribute__((ext_vector_type(2)));
typedef short s16x4 __attribute__((ext_vector_type(4)));
typedef __bf16 bf16x8 __attribute__((ext_vector_type(8)));
typedef __bf16 bf16x2 __attribute__((ext_vector_type(2)));
#define LAS __attribute__((address_space(3)))
#define DEV __device__ __forceinline__

constexpr int L = 2048, D = 512, NB = 256, NT = 512;
constexpr u32 O_WQ = 0;
constexpr u32 O_WO = O_WQ + 1536 * 512 * 2;
constexpr u32 O_KR = O_WO + 512 * 512 * 2;
constexpr u32 KR_STR = 2064 * 2;
constexpr u32 XSZ = L * D * 2;
constexpr u32 O_X0 = O_KR + 512 * KR_STR;
constexpr u32 O_ST0 = O_X0 + XSZ;
constexpr u32 O_X1 = O_ST0 + L * 8;
constexpr u32 O_X2 = O_X1 + XSZ;
constexpr u32 O_X3 = O_X2 + XSZ;
constexpr u32 O_ST2 = O_X3 + XSZ;
constexpr u32 O_PQ = O_ST2 + 4 * L * 8;
constexpr u32 O_KVP = O_PQ + 2 * XSZ;
constexpr u32 KVP_SZ = 32 * 32 * 1024;
constexpr u32 O_ZP = O_KVP + 2 * KVP_SZ;
constexpr u32 ZP_SZ = 32 * 32 * 64;
constexpr u32 O_KVT = O_ZP + 2 * ZP_SZ;
constexpr u32 KVT_SZ = 32 * 512;
constexpr u32 O_ZT = O_KVT + 2 * KVT_SZ;
constexpr u32 ZT_SZ = 32 * 32;
constexpr u32 O_FLG = O_ZT + 2 * ZT_SZ;
constexpr u32 O_EPO = O_FLG + 1024;
constexpr u32 O_END = O_EPO + 128;

struct Params {
  const u32* u; const unsigned short* logA; const unsigned short* Aim; const unsigned short* Cre; const unsigned short* Cim;
  const unsigned short* logdt; const unsigned short* Dskip; const unsigned short* wqkv; const unsigned short* wo;
  char* ws; unsigned short* out;
};

DEV float bf2f(u32 h) { return __builtin_bit_cast(float, h << 16); }
DEV float bflo(u32 w) { return __builtin_bit_cast(float, w << 16); }
DEV float bfhi(u32 w) { return __builtin_bit_cast(float, w & 0xffff0000u); }
DEV u32 pk2(float lo, float hi) { f32x2 f = {lo, hi}; return __builtin_bit_cast(u32, __builtin_convertvector(f, bf16x2)); }
DEV float rbf(float x) { return bflo(pk2(x, x)); }
DEV f32x4 mfma32(u32x4 a, u32x4 b, f32x4 c) {
  return __builtin_amdgcn_mfma_f32_16x16x32_bf16(__builtin_bit_cast(bf16x8, a), __builtin_bit_cast(bf16x8, b), c, 0, 0, 0);
}
DEV f32x4 mfma16(u32x2 a, u32x2 b, f32x4 c) {
  return __builtin_amdgcn_mfma_f32_16x16x16bf16_1k(__builtin_bit_cast(s16x4, a), __builtin_bit_cast(s16x4, b), c, 0, 0, 0);
}
DEV float dot2(u32 a, u32 b, float c) { return __builtin_amdgcn_fdot2_f32_bf16(__builtin_bit_cast(bf16x2, a), __builtin_bit_cast(bf16x2, b), c, false); }
typedef __amdgpu_buffer_rsrc_t rsrc_t;
DEV rsrc_t mk_rsrc(const void* p, u32 bytes) { return __builtin_amdgcn_make_buffer_rsrc((void*)p, (short)0, (int)bytes, 0x00020000); }
template <int AUX> DEV u32x4 ld16(rsrc_t r, u32 off) { return __builtin_amdgcn_raw_buffer_load_b128(r, off, 0, AUX); }
template <int AUX> DEV u32x2 ld8(rsrc_t r, u32 off) { return __builtin_amdgcn_raw_buffer_load_b64(r, off, 0, AUX); }
template <int AUX> DEV u32 ld4(rsrc_t r, u32 off) { return __builtin_amdgcn_raw_buffer_load_b32(r, off, 0, AUX); }
template <int AUX> DEV void st16(rsrc_t r, u32 off, u32x4 v) { __builtin_amdgcn_raw_buffer_store_b128(v, r, off, 0, AUX); }
template <int AUX> DEV void st8(rsrc_t r, u32 off, u32x2 v) { __builtin_amdgcn_raw_buffer_store_b64(v, r, off, 0, AUX); }
template <int AUX> DEV void st4(rsrc_t r, u32 off, u32 v) { __builtin_amdgcn_raw_buffer_store_b32(v, r, off, 0, AUX); }
DEV u32x4 lds_ld16(const char* s, u32 off) { return *(const LAS u32x4*)((const LAS char*)s + off); }
DEV u32x2 lds_ld8(const char* s, u32 off) { return *(const LAS u32x2*)((const LAS char*)s + off); }
DEV u32 lds_ld4(const char* s, u32 off) { return *(const LAS u32*)((const LAS char*)s + off); }
DEV void lds_st16(char* s, u32 off, u32x4 v) { *(LAS u32x4*)((LAS char*)s + off) = v; }
DEV void lds_st8(char* s, u32 off, u32x2 v) { *(LAS u32x2*)((LAS char*)s + off) = v; }
DEV void lds_st4(char* s, u32 off, u32 v) { *(LAS u32*)((LAS char*)s + off) = v; }
DEV u32x2 lds_tr(const char* s, u32 off) {
  return __builtin_bit_cast(u32x2, __builtin_amdgcn_ds_read_tr16_b64_v4i16((LAS s16x4*)((LAS char*)s + off)));
}
DEV float f_(u32 x) { return __builtin_bit_cast(float, x); }
DEV u32 u_(float x) { return __builtin_bit_cast(u32, x); }
DEV float xor16(float v) { return __shfl_xor(v, 16); }
DEV float xor32(float v) { return __shfl_xor(v, 32); }
DEV void bar() { asm volatile("s_waitcnt lgkmcnt(0)\n\ts_barrier" ::: "memory"); }
DEV int otid() { int t = threadIdx.x; asm volatile("" : "+v"(t)); return t; }
DEV void sfence() { __builtin_amdgcn_sched_barrier(0); }
constexpr u32 SENT = 0xffffffffu;
constexpr int SC1 = 16;
#define GC 8
#define GQ 0
#define GO 1
#define WC 0
#define WQ1 0
#define WO1 0
#define SLP 1
constexpr int SPIN_CAP = 1 << 14;
DEV bool isbad(u32x4 v) { return max(max(v[0], v[1]), max(v[2], v[3])) == SENT; }
DEV bool isbad(u32x2 v) { return max(v[0], v[1]) == SENT; }
DEV bool anyb(bool b) { return __builtin_amdgcn_ballot_w64(b) != 0ull; }
template <int N, class F> DEV void repoll16(rsrc_t r, F off, u32x4 (&v)[N]) {
  for (int spin = 0; spin < SPIN_CAP; ++spin) {
    u32 mask = 0;
#pragma unroll
    for (int i = 0; i < N; ++i) mask |= anyb(isbad(v[i])) ? (1u << i) : 0u;
    if (!mask) break;
    __builtin_amdgcn_s_sleep(SLP);
#pragma unroll
    for (int i = 0; i < N; ++i) if (mask & (1u << i)) v[i] = ld16<SC1>(r, off(i));
  }
}
template <int N, int G, bool ONEW, class F> DEV void poll16(rsrc_t r, F off, u32x4 (&v)[N]) {
  if (G >= 0) {
    if (!ONEW || threadIdx.x < 64) {
      v[G >= 0 ? G : 0] = ld16<SC1>(r, off(G));
      for (int spin = 0; spin < SPIN_CAP; ++spin) {
        if (!anyb(isbad(v[G >= 0 ? G : 0]))) break;
        __builtin_amdgcn_s_sleep(SLP);
        v[G >= 0 ? G : 0] = ld16<SC1>(r, off(G));
      }
    }
    if (ONEW) asm volatile("s_barrier" ::: "memory");
  }
#pragma unroll
  for (int i = 0; i < N; ++i) if (i != G || ONEW) v[i] = ld16<SC1>(r, off(i));
  repoll16<N>(r, off, v);
}
DEV int permk(int g, int j) { return j < 4 ? 4 * g + j : 16 + 4 * g + (j - 4); }
DEV void ln_stats(float sum, float sq, float& m, float& rinv) {
  float mean = sum * (1.0f / 512.0f);
  float var = fmaxf(sq * (1.0f / 512.0f) - mean * mean, 0.0f);
  m = rbf(mean);
  float den = rbf(__builtin_amdgcn_sqrtf(rbf(rbf(var) + 1e-5f)));
  rinv = __builtin_amdgcn_rcpf(den);
}
DEV f32x2 unpk(u32 w) { return f32x2{bflo(w), bfhi(w)}; }
DEV f32x2 rbf2(f32x2 v) { return unpk(pk2(v[0], v[1])); }
DEV f32x2 lo2(f32x4 v) { return f32x2{v[0], v[1]}; }
DEV f32x2 hi2(f32x4 v) { return f32x2{v[2], v[3]}; }
DEV u32 phi2(f32x2 a) {
  f32x2 t = rbf2(a), s = t * 1.4426950408889634f, p = t + 1.0f;
  f32x2 e = {__builtin_amdgcn_exp2f(s[0]), __builtin_amdgcn_exp2f(s[1])};
  f32x2 q = {t[0] > 0.0f ? p[0] : e[0], t[1] > 0.0f ? p[1] : e[1]};
  q = rbf2(q) + 1e-6f;
  return pk2(q[0], q[1]);
}
DEV float phi(float t) { float p = t > 0.0f ? t + 1.0f : __expf(t); return rbf(rbf(p) + 1e-6f); }
#define STAMP(i)
#define STAMP_WS(p)
#define STAMP_ARG
#define STAMP_PASS
constexpr u32 KA_ROW = 528;
constexpr u32 KA_TAB = 16 * KA_ROW;
constexpr u32 KA_G = 0, KA_E = 8 * KA_TAB;
constexpr u32 KA_KC = 16 * KA_TAB;
constexpr u32 KA_XT = KA_KC + 2 * 656 * 4;
constexpr u32 KA_LDS = KA_XT + 8 * 1040;
constexpr u32 KA_WIN = 0, KA_WSTR = 2064 * 16;
constexpr u32 KA_R = 100352;
#define KST 16
typedef f32x2 cf;
DEV cf cmul(cf a, cf b) { return f32x2{a[0], a[0]} * b + f32x2{a[1], a[1]} * f32x2{-b[1], b[0]}; }
DEV cf cexp_pow(float are, float aim, int k) {
  float s = (float)(1 << k);
  float mag = __expf(are * s);
  double rev = (double)aim * (0.15915494309189535 * (double)(1 << k));
  float fr = (float)(rev - __builtin_floor(rev));
  return f32x2{__builtin_amdgcn_cosf(fr), __builtin_amdgcn_sinf(fr)} * mag;
}
DEV void phase_A(const Params& p, rsrc_t rw, char* sm, int d0, u32x4& xv0, u32x4& s0, u32x4& s1 STAMP_ARG) {
  const int tid = otid(), b = blockIdx.x, lane = tid & 63, w = tid >> 6;
  const int n = tid & 127, ch = (tid >> 7) & 1, sih = tid >> 8;
  const int d = d0 + ch;
  const u32 rlA = p.logA[d * 128 + n], rAi = p.Aim[d * 128 + n], rCr = p.Cre[d * 128 + n], rCi = p.Cim[d * 128 + n], rdt = p.logdt[d];
  const u32 tag = ld4<SC1>(rw, O_EPO) + 1u;
  sfence();
  const int rg = (b & 7) * 32 + (b >> 3);
  const int row = rg * 8 + w;
  u32x4 xv = *(const u32x4*)(p.u + (size_t)row * 256 + lane * 4);
  u32 wv[8]; u32 wdst;
  {
    int g = b * NT + tid, ln = g & 63, lq = ln >> 4;
    const unsigned short* src; int ld, ks;
    if (g < 98304) {
      int f = g >> 6, ty = f % 3, f2 = f / 3, ks8 = f2 & 7, kh = (f2 >> 3) & 1, hh = (f2 >> 4) & 3, cb = f2 >> 6;
      ks = kh * 8 + ks8; src = p.wqkv + ty * 512 + (4 * cb + hh) * 16 + (ln & 15); ld = 1536; wdst = O_WQ + (u32)g * 16;
    } else {
      int g2 = g - 98304, f = g2 >> 6; ks = f & 15; int ww = (f >> 4) & 7, cb4 = f >> 7;
      src = p.wo + 128 * cb4 + 16 * ww + (ln & 15); ld = 512; wdst = O_WO + (u32)g2 * 16;
    }
#pragma unroll
    for (int j = 0; j < 8; ++j) wv[j] = src[(size_t)(32 * ks + permk(lq, j)) * ld];
  }
  {
    const u32 g = (u32)(b * NT + tid) * 16;
    const u32x4 sv = {SENT, SENT, SENT, SENT};
    st16<KST>(rw, O_X1 + g, sv);
    if (g < O_FLG - O_ZP) st16<KST>(rw, O_ZP + g, sv);
    if (g < 4 * L * 8) st16<KST>(rw, O_ST2 + g, sv);
  }
  sfence();
  const float lA = bf2f(rlA), Ai = bf2f(rAi), Cr = bf2f(rCr), Ci = bf2f(rCi), dt = rbf(expf(bf2f(rdt)));
  float are = -rbf(expf(lA)) * dt, aim = Ai * dt;
  cf C = {Cr * dt, Ci * dt};
#pragma unroll
  for (int t = 0; t < 2; ++t) {
    const int si = sih + 2 * t;
    const u32 tb = (u32)(ch * 4 + si) * KA_TAB + n * 4;
    cf v0 = cexp_pow(are, aim, si), v1 = cmul(v0, v0), v2 = cmul(v1, v1), v3 = cmul(v2, v2);
    cf e[8]; e[0] = f32x2{1.f, 0.f}; e[1] = v0; e[2] = v1; e[3] = cmul(v1, v0); e[4] = v2;
    e[5] = cmul(v2, v0); e[6] = cmul(v2, v1); e[7] = cmul(v2, e[3]);
#pragma unroll
    for (int j = 0; j < 8; ++j) {
      lds_st4(sm, KA_E + tb + j * KA_ROW, pk2(e[j][0], e[j][1]));
      cf f = cmul(e[j], v3);
      lds_st4(sm, KA_E + tb + (j + 8) * KA_ROW, pk2(f[0], f[1]));
    }
    cf g0 = cexp_pow(are, aim, 4 + si), g1 = cmul(g0, g0), g2 = cmul(g1, g1), g3 = cmul(g2, g2);
    cf h[8]; h[0] = (si == 0) ? C : cmul(C, g3);
    h[1] = cmul(h[0], g0); h[2] = cmul(h[0], g1); h[3] = cmul(h[1], g1);
#pragma unroll
    for (int j = 0; j < 4; ++j) h[4 + j] = cmul(h[j], g2);
#pragma unroll
    for (int j = 0; j < 8; ++j) {
      lds_st4(sm, KA_G + tb + j * KA_ROW, pk2(h[j][0], -h[j][1]));
      cf f = cmul(h[j], g3);
      lds_st4(sm, KA_G + tb + (j + 8) * KA_ROW, pk2(f[0], -f[1]));
    }
  }
  sfence();
  STAMP(24);
  {
    float s = 0.f, q = 0.f;
#pragma unroll
    for (int j = 0; j < 4; ++j) { s = dot2(xv[j], 0x3f803f80u, s); q = dot2(xv[j], xv[j], q); }
#pragma unroll
    for (int o = 1; o < 64; o <<= 1) { s += __shfl_xor(s, o); q += __shfl_xor(q, o); }
    if (lane == 0) { u32x2 v = {u_(s), u_(q)}; st8<KST>(rw, O_ST0 + row * 8, v); }
    lds_st16(sm, KA_XT + w * 1040 + lane * 16, xv);
  }
  { u32x4 v = {wv[0] | (wv[1] << 16), wv[2] | (wv[3] << 16), wv[4] | (wv[5] << 16), wv[6] | (wv[7] << 16)}; st16<KST>(rw, wdst, v); }
  bar();
  {
    u32 v[8];
#pragma unroll
    for (int r = 0; r < 8; ++r) v[r] = *(const LAS unsigned short*)((LAS char*)sm + KA_XT + r * 1040 + tid * 2);
    u32x4 o = {v[0] | (v[1] << 16), v[2] | (v[3] << 16), v[4] | (v[5] << 16), v[6] | (v[7] << 16)};
    st16<KST>(rw, O_X0 + (((rg >> 2) * 512 + tid) * 32 + (rg & 3) * 8) * 2, o);
  }
  sfence();
  STAMP(25);
  {
    const int wc = w >> 2, si = w & 3, lq = lane >> 4, r16 = lane & 15;
    const u32 base = (u32)(wc * 4 + si) * KA_TAB + r16 * KA_ROW + lq * 16;
    f32x4 acc = {0.f, 0.f, 0.f, 0.f};
#pragma unroll
    for (int ks = 0; ks < 8; ++ks) acc = mfma32(lds_ld16(sm, KA_G + base + ks * 64), lds_ld16(sm, KA_E + base + ks * 64), acc);
#pragma unroll
    for (int r = 0; r < 4; ++r) {
      int i = 4 * lq + r;
      int idx = si == 0 ? 16 * i + r16 : 256 + (si - 1) * 128 + 16 * i + r16;
      bool ok = si == 0 || i < 8 || (si == 3 && i == 8 && r16 == 0);
      if (ok) lds_st4(sm, KA_KC + (wc * 656 + idx) * 4, u_(2.0f * acc[r]));
    }
  }
  asm volatile("s_waitcnt vmcnt(0)" ::: "memory");
  bar();
  if (tid == 0) st4<SC1>(rw, O_FLG + b * 4, tag);
  STAMP(26);
  const int c2 = tid >> 8, m = tid & 255;
  {
    u32 o[4];
#pragma unroll
    for (int j = 0; j < 4; ++j) {
      float kv[2];
#pragma unroll
      for (int h = 0; h < 2; ++h) {
        int l = 2047 - (8 * m + 2 * j + h);
        int e = l < 256 ? 0 : (l < 512 ? 1 : (l < 1024 ? 2 : 3));
        int mm = (l + ((1 << e) >> 1) - 1 + ((l >> e) & 1)) >> e;
        int idx = e == 0 ? l : 256 + (e - 1) * 128 + (mm - 128);
        kv[h] = f_(lds_ld4(sm, KA_KC + (c2 * 656 + idx) * 4));
      }
      o[j] = pk2(kv[0], kv[1]);
    }
    lds_st16(sm, KA_R + c2 * KR_STR + m * 16, u32x4{o[0], o[1], o[2], o[3]});
    if (m < 2) lds_st16(sm, KA_R + c2 * KR_STR + 4096 + m * 16, u32x4{0u, 0u, 0u, 0u});
  }
  if (w == 0) {
    u32x4 fl = ld16<SC1>(rw, O_FLG + lane * 16);
    for (int spin = 0; spin < SPIN_CAP; ++spin) {
      if (!anyb(fl[0] != tag || fl[1] != tag || fl[2] != tag || fl[3] != tag)) break;
      __builtin_amdgcn_s_sleep(SLP);
      fl = ld16<SC1>(rw, O_FLG + lane * 16);
    }
    if (b == 0 && lane == 0) st4<SC1>(rw, O_EPO, tag);
  }
  bar();
  STAMP(27);
  xv0 = ld16<SC1>(rw, O_X0 + (((m >> 2) * 512 + d0 + c2) * 32 + (m & 3) * 8) * 2);
  s0 = ld16<SC1>(rw, O_ST0 + tid * 32); s1 = ld16<SC1>(rw, O_ST0 + tid * 32 + 16);
#pragma unroll
  for (int i = 0; i < 4; ++i) {
    const u32 rb_ = KA_R + c2 * KR_STR + (m + 256 * i) * 4;
    u32 a[5];
#pragma unroll
    for (int j = 0; j < 5; ++j) a[j] = lds_ld4(sm, rb_ + j * 4);
    const u32 gb = KA_WIN + c2 * KA_WSTR + (m + 256 * i) * 32;
    lds_st16(sm, gb, u32x4{a[0], a[1], a[2], a[3]});
    lds_st16(sm, gb + 16, u32x4{__builtin_amdgcn_alignbit(a[1], a[0], 16), __builtin_amdgcn_alignbit(a[2], a[1], 16),
                                __builtin_amdgcn_alignbit(a[3], a[2], 16), __builtin_amdgcn_alignbit(a[4], a[3], 16)});
  }
  if (m < 16) lds_st16(sm, KA_WIN + c2 * KA_WSTR + (2048 + m) * 16, u32x4{0u, 0u, 0u, 0u});
}
#define PF 0
constexpr u32 P_G = 0, P_GSTR = 2064 * 16;
constexpr u32 DB = 2 * P_GSTR;
constexpr u32 P_CS = 158720;
constexpr u32 P_LDS = P_CS + 8 * 3 * 16 * 4;
constexpr u32 C_U = DB, C_ST = C_U + 2 * 4608, C_RED = C_ST + L * 8;
constexpr u32 Q_A = DB, Q_EX = DB, Q_SP = DB + 65536, Q_MR = Q_SP + 1024;
constexpr u32 O_OF = DB, O_T = DB + 32768, O_PS = O_T + 128 * 144;
constexpr u32 R_B = DB;
static_assert(KA_LDS <= P_CS && KA_WIN == P_G && KA_WSTR == P_GSTR && KA_R + 2 * KR_STR <= KA_KC && KA_R >= C_RED && C_RED + 65536 <= P_CS && Q_MR + 8 * 512 <= P_CS && P_LDS <= 163840, "LDS map");

DEV void fill16(rsrc_t rw, u32 off) { st16<SC1>(rw, off, u32x4{SENT, SENT, SENT, SENT}); }
template <int NP, bool POLL>
DEV void phase_C(rsrc_t rw, char* sm, u32x4 xv0, u32x4 s0, u32x4 s1, u32x4 (&W)[8][3], int cb, int d0, float dsk, u32 o_in, u32 o_st, u32 o_out, int sb STAMP_ARG) {
  const int tid = otid(), lane = tid & 63, w = tid >> 6, ch = tid >> 8, m = tid & 255;
  const u32 xoff = (((m >> 2) * 512 + d0 + ch) * 32 + (m & 3) * 8) * 2;
  u32x4 xv;
  {
    u32x4 sv[2 * NP + 1];
    auto off = [&](int i) -> u32 { return i == 2 * NP ? o_in + xoff : o_st + (i >> 1) * L * 8 + tid * 32 + (i & 1) * 16; };
    if (POLL) poll16<2 * NP + 1, (POLL ? GC : -1), WC>(rw, off, sv);
    else {
      sv[0] = s0; sv[1] = s1; sv[2 * NP] = xv0;
      const u32 fg = (u32)(blockIdx.x * NT + tid) * 16;
      fill16(rw, O_PQ + fg); fill16(rw, O_KVP + fg);
    }
    xv = sv[2 * NP];
    float mr[8];
#pragma unroll
    for (int r = 0; r < 4; ++r) {
      float s = 0.f, q2 = 0.f;
#pragma unroll
      for (int q = 0; q < NP; ++q) { s += f_(sv[2 * q + (r >> 1)][(r & 1) * 2]); q2 += f_(sv[2 * q + (r >> 1)][(r & 1) * 2 + 1]); }
      ln_stats(s, q2, mr[2 * r], mr[2 * r + 1]);
    }
    lds_st16(sm, C_ST + tid * 32, u32x4{u_(mr[0]), u_(mr[2]), u_(mr[1]), u_(mr[3])});
    lds_st16(sm, C_ST + tid * 32 + 16, u32x4{u_(mr[4]), u_(mr[6]), u_(mr[5]), u_(mr[7])});
    if (tid < 64) lds_st16(sm, C_U + (tid >> 5) * 4608 + ((tid >> 4) & 1) * 2304 + (tid & 15) * 16, u32x4{0u, 0u, 0u, 0u});
  }
  asm volatile("s_waitcnt vmcnt(0)" ::: "memory");
  bar();
  STAMP(sb + 1);
  u32x4 uv;
#pragma unroll
  for (int j = 0; j < 4; ++j) {
    u32x4 s = lds_ld16(sm, C_ST + (8 * m + 2 * j) * 8);
    f32x2 t = rbf2(unpk(xv[j]) - f32x2{f_(s[0]), f_(s[1])}) * f32x2{f_(s[2]), f_(s[3])};
    uv[j] = pk2(t[0], t[1]);
  }
  lds_st16(sm, C_U + ch * 4608 + (m & 1) * 2304 + (16 + (m >> 1)) * 16, uv);
  bar();
  {
    const int wc = w >> 2, w4 = w & 3, lq = lane >> 4, r16 = lane & 15;
    const int j0 = (lq & 1) * 16 + (lq >> 1) * 8, rho = 2 * w4;
    const u32 ub = C_U + wc * 4608 + (lq >> 1) * 2304 + (15 - 2 * rho + r16 + (lq & 1)) * 16;
    const u32 gb = P_G + wc * P_GSTR + (2031 - 32 * (rho + 1) - 256 * 7 - r16 + j0) * 16;
    f32x4 acc[8];
#pragma unroll
    for (int c = 0; c < 8; ++c) acc[c] = f32x4{0.f, 0.f, 0.f, 0.f};
    u32x4 B0[8], A0[8], B1[8], A1[8];
#pragma unroll
    for (int k = 0; k < 8; ++k) { A0[k] = lds_ld16(sm, gb + 512 + (7 - k) * 4096); B0[k] = lds_ld16(sm, ub + k * 256); }
    sfence();
    const u32 wb = O_WQ + (u32)(cb * 8 + w) * 24576 + lane * 16;
#pragma unroll
    for (int g = 0; g < 8; ++g) {
      A1[g] = lds_ld16(sm, gb + (7 - g) * 4096); B1[g] = lds_ld16(sm, ub - 32 + g * 256);
#pragma unroll
      for (int i = 2 * g; i < 2 * g + 2; ++i) W[i / 3][i % 3] = ld16<SC1>(rw, wb + i * 1024);
      sfence();
#pragma unroll
      for (int c = 7; c >= g; --c) acc[c] = mfma32(A0[g], B0[c - g], acc[c]);
      sfence();
    }
#pragma unroll
    for (int g = 0; g < 8; ++g) {
      W[(16 + g) / 3][(16 + g) % 3] = ld16<SC1>(rw, wb + (16 + g) * 1024);
      sfence();
#pragma unroll
      for (int c = g; c < 8; ++c) acc[c] = mfma32(A1[g], B1[c - g], acc[c]);
      sfence();
    }
#pragma unroll
    for (int c = 0; c < 8; ++c) lds_st16(sm, C_RED + ((wc * 4 + w4) * 8 + c) * 1024 + lane * 16, __builtin_bit_cast(u32x4, acc[c]));
  }
  bar();
  STAMP(sb + 2);
  {
    const int c = m >> 5, a = (m & 31) >> 1, lq0 = (m & 1) * 2;
    f32x4 y0 = {0.f, 0.f, 0.f, 0.f}, y1 = y0;
#pragma unroll
    for (int q = 0; q < 4; ++q) {
      const u32 o = C_RED + ((ch * 4 + q) * 8 + c) * 1024 + (lq0 * 16 + a) * 16;
      y0 += __builtin_bit_cast(f32x4, lds_ld16(sm, o)); y1 += __builtin_bit_cast(f32x4, lds_ld16(sm, o + 256));
    }
    u32x4 ov;
#pragma unroll
    for (int j = 0; j < 4; ++j) {
      f32x2 y = j == 0 ? lo2(y0) : j == 1 ? hi2(y0) : j == 2 ? lo2(y1) : hi2(y1);
      f32x2 r = unpk(xv[j]) + rbf2(rbf2(y) + rbf2(unpk(uv[j]) * dsk));
      ov[j] = pk2(r[0], r[1]);
    }
    st16<SC1>(rw, o_out + xoff, ov);
  }
  bar();
}

DEV void q_colsum(char* sm, const u32x4 (&W)[8][3]) {
  const int tid = otid(), lane = tid & 63, w = tid >> 6;
#pragma unroll
  for (int ty = 0; ty < 3; ++ty) {
    float s = 0.f;
#pragma unroll
    for (int ks = 0; ks < 8; ++ks)
#pragma unroll
      for (int j = 0; j < 4; ++j) s = dot2(W[ks][ty][j], 0x3f803f80u, s);
    s += xor16(s); s += xor32(s);
    if (lane < 16) lds_st4(sm, P_CS + ((w * 3 + ty) * 16 + lane) * 4, u_(s));
  }
}
template <bool FIRST>
DEV void phase_Q(rsrc_t rw, char* sm, const u32x4 (&W)[8][3], u32x4 (&Wo)[16], int rb, int cb, int cb4, u32 o_x, u32 o_pq, u32 o_kvp, u32 o_zp,
                 int sb STAMP_ARG) {
  const int tid = otid(), lane = tid & 63, w = tid >> 6, hh = w >> 1, kh = w & 1, lq = lane >> 4, r16 = lane & 15;
  {
    u32x4 av[8];
    poll16<8, GQ, WQ1>(rw, [&](int i) -> u32 { return o_x + (u32)rb * 65536 + (tid + i * 512) * 16; }, av);
    const int chn = tid >> 2, pc = tid & 3, f = (chn >> 1) & 3;
#pragma unroll
    for (int i = 0; i < 8; ++i)
      lds_st16(sm, Q_A + (chn + 128 * (i & 3)) * 128 + ((((i >> 2) * 2 + (pc >> 1)) ^ f) * 32) + (pc & 1) * 16, av[i]);
  }
  bar();
  STAMP(sb + 1);
  f32x4 acc[4][3];
#pragma unroll
  for (int rt = 0; rt < 4; ++rt)
#pragma unroll
    for (int ty = 0; ty < 3; ++ty) acc[rt][ty] = f32x4{0.f, 0.f, 0.f, 0.f};
  const int chl = 4 * lq + (r16 >> 2), f = (chl >> 1) & 3;
  u32 ab[4];
#pragma unroll
  for (int rt = 0; rt < 4; ++rt) ab[rt] = Q_A + (kh * 256 + chl) * 128 + ((rt ^ f) * 32) + (r16 & 3) * 8;
  const u32 abs_ = Q_A + (kh * 256 + chl) * 128 + ((hh ^ f) * 32) + (r16 & 3) * 8;
  float ssum = 0.f, ssq = 0.f;
  const u32 wob = O_WO + (u32)(cb4 * 8 + w) * 16384 + lane * 16;
  {
    u32x2 xa[2][4][2], xs[2][2];
#pragma unroll
    for (int rt = 0; rt < 4; ++rt) { xa[0][rt][0] = lds_tr(sm, ab[rt]); xa[0][rt][1] = lds_tr(sm, ab[rt] + 2048); }
    xs[0][0] = lds_tr(sm, abs_); xs[0][1] = lds_tr(sm, abs_ + 2048);
#pragma unroll
    for (int ks = 0; ks < 8; ++ks) {
      const int cu = ks & 1, nx = cu ^ 1;
      if (ks < 7) {
#pragma unroll
        for (int rt = 0; rt < 4; ++rt) { xa[nx][rt][0] = lds_tr(sm, ab[rt] + (ks + 1) * 4096); xa[nx][rt][1] = lds_tr(sm, ab[rt] + (ks + 1) * 4096 + 2048); }
        xs[nx][0] = lds_tr(sm, abs_ + (ks + 1) * 4096); xs[nx][1] = lds_tr(sm, abs_ + (ks + 1) * 4096 + 2048);
      }
      Wo[2 * ks] = ld16<SC1>(rw, wob + 2 * ks * 1024); Wo[2 * ks + 1] = ld16<SC1>(rw, wob + (2 * ks + 1) * 1024);
      if (FIRST && ks == 1) fill16(rw, O_X2 + (u32)(blockIdx.x * NT + tid) * 16);
      if (FIRST && ks == 4) fill16(rw, O_X3 + (u32)(blockIdx.x * NT + tid) * 16);
      sfence();
#pragma unroll
      for (int rt = 0; rt < 4; ++rt) {
        u32x4 X = {xa[cu][rt][0][0], xa[cu][rt][0][1], xa[cu][rt][1][0], xa[cu][rt][1][1]};
        acc[rt][0] = mfma32(W[ks][0], X, acc[rt][0]);
        acc[rt][1] = mfma32(X, W[ks][1], acc[rt][1]);
        acc[rt][2] = mfma32(X, W[ks][2], acc[rt][2]);
      }
#pragma unroll
      for (int j = 0; j < 2; ++j)
#pragma unroll
        for (int e = 0; e < 2; ++e) { ssum = dot2(xs[cu][j][e], 0x3f803f80u, ssum); ssq = dot2(xs[cu][j][e], xs[cu][j][e], ssq); }
      sfence();
    }
  }
  ssum += xor16(ssum); ssum += xor32(ssum); ssq += xor16(ssq); ssq += xor32(ssq);
  if (lane < 16) lds_st8(sm, Q_SP + (kh * 64 + hh * 16 + lane) * 8, u32x2{u_(ssum), u_(ssq)});
  bar();
  const u32 ex = Q_EX + hh * 12288 + lane * 16;
  if (kh == 0) {
#pragma unroll
    for (int rt = 0; rt < 4; ++rt) lds_st16(sm, ex + rt * 1024, __builtin_bit_cast(u32x4, acc[rt][0]));
  } else {
#pragma unroll
    for (int rt = 0; rt < 4; ++rt) {
      lds_st16(sm, ex + (4 + rt * 2) * 1024, __builtin_bit_cast(u32x4, acc[rt][1]));
      lds_st16(sm, ex + (5 + rt * 2) * 1024, __builtin_bit_cast(u32x4, acc[rt][2]));
    }
  }
  {
    u32x2 p0 = lds_ld8(sm, Q_SP + lane * 8), p1 = lds_ld8(sm, Q_SP + (64 + lane) * 8);
    float mean, rinv; ln_stats(f_(p0[0]) + f_(p1[0]), f_(p0[1]) + f_(p1[1]), mean, rinv);
    lds_st4(sm, Q_MR + w * 512 + lane * 4, u_(mean)); lds_st4(sm, Q_MR + w * 512 + 256 + lane * 4, u_(rinv));
  }
  bar();
  STAMP(sb + 2);
  const int h = cb * 4 + hh;
  const u32 mrb = Q_MR + w * 512, csb = P_CS + hh * 2 * 3 * 16 * 4;
  if (kh == 1) {
    const f32x4 cs = __builtin_bit_cast(f32x4, lds_ld16(sm, csb + 16 * lq)) + __builtin_bit_cast(f32x4, lds_ld16(sm, csb + 192 + 16 * lq));
#pragma unroll
    for (int rp = 0; rp < 2; ++rp) {
      u32x4 ov;
#pragma unroll
      for (int t = 0; t < 2; ++t) {
        const int rt = rp * 2 + t;
        f32x4 a = acc[rt][0] + __builtin_bit_cast(f32x4, lds_ld16(sm, ex + rt * 1024));
        const float mean = f_(lds_ld4(sm, mrb + (rt * 16 + r16) * 4)), rinv = f_(lds_ld4(sm, mrb + 256 + (rt * 16 + r16) * 4));
        ov[2 * t] = phi2((lo2(a) - lo2(cs) * mean) * rinv); ov[2 * t + 1] = phi2((hi2(a) - hi2(cs) * mean) * rinv);
      }
      st16<SC1>(rw, o_pq + (((rb * 2 + rp) * 32 + h) * 64 + lane) * 16, ov);
    }
  } else {
    const float csk = f_(lds_ld4(sm, csb + 64 + r16 * 4)) + f_(lds_ld4(sm, csb + 192 + 64 + r16 * 4));
    const float csv = f_(lds_ld4(sm, csb + 128 + r16 * 4)) + f_(lds_ld4(sm, csb + 192 + 128 + r16 * 4));
    f32x4 kv = {0.f, 0.f, 0.f, 0.f}, zz = kv;
#pragma unroll
    for (int rp = 0; rp < 2; ++rp) {
      u32x4 pk, vv;
#pragma unroll
      for (int t = 0; t < 2; ++t) {
        const int rt = rp * 2 + t;
        f32x4 ak = acc[rt][1] + __builtin_bit_cast(f32x4, lds_ld16(sm, ex + (4 + rt * 2) * 1024));
        f32x4 av = acc[rt][2] + __builtin_bit_cast(f32x4, lds_ld16(sm, ex + (5 + rt * 2) * 1024));
        const f32x4 mean = __builtin_bit_cast(f32x4, lds_ld16(sm, mrb + (rt * 16 + 4 * lq) * 4));
        const f32x4 rinv = __builtin_bit_cast(f32x4, lds_ld16(sm, mrb + 256 + (rt * 16 + 4 * lq) * 4));
        pk[2 * t] = phi2((lo2(ak) - lo2(mean) * csk) * lo2(rinv)); pk[2 * t + 1] = phi2((hi2(ak) - hi2(mean) * csk) * hi2(rinv));
        f32x2 v0 = (lo2(av) - lo2(mean) * csv) * lo2(rinv), v1 = (hi2(av) - hi2(mean) * csv) * hi2(rinv);
        vv[2 * t] = pk2(v0[0], v0[1]); vv[2 * t + 1] = pk2(v1[0], v1[1]);
      }
      kv = mfma32(pk, vv, kv);
      zz = mfma32(pk, u32x4{0x3f803f80u, 0x3f803f80u, 0x3f803f80u, 0x3f803f80u}, zz);
    }
    st16<SC1>(rw, o_kvp + ((rb * 32 + h) * 64 + lane) * 16, __builtin_bit_cast(u32x4, kv));
    if (r16 == 0) st16<SC1>(rw, o_zp + (rb * 32 + h) * 64 + lq * 16, __builtin_bit_cast(u32x4, zz));
  }
  bar();
}

DEV void phase_R(rsrc_t rw, char* sm, int b, u32 o_kvp, u32 o_zp, u32 o_kvt, u32 o_zt) {
  const int tid = otid(), h = b >> 3, j = b & 7, rb = tid >> 4, q = tid & 15;
  u32x4 v[1];
  const u32 off = q < 8 ? o_kvp + ((rb * 32 + h) * 64 + 8 * j + q) * 16 : o_zp + (rb * 32 + h) * 64 + (j >> 1) * 16;
  poll16<1, -1, false>(rw, [&](int) -> u32 { return off; }, v);
  asm volatile("s_waitcnt vmcnt(0)" ::: "memory");
  if (q < 8) lds_st16(sm, R_B + (rb * 40 + q * 4) * 4, v[0]);
  else if (q == 8) lds_st8(sm, R_B + (rb * 40 + 32) * 4, (j & 1) ? u32x2{v[0][2], v[0][3]} : u32x2{v[0][0], v[0][1]});
  bar();
  if (tid < 64) {
    const int e = tid < 34 ? tid : 0;
    float s = 0.f;
#pragma unroll
    for (int r = 0; r < 32; ++r) s += f_(lds_ld4(sm, R_B + (r * 40 + e) * 4));
    float o = __shfl_down(s, 1), o2 = __shfl_down(s, 2), o3 = __shfl_down(s, 3);
    if (tid < 32 && (tid & 3) == 0) st8<SC1>(rw, o_kvt + (h * 64 + 8 * j + (tid >> 2)) * 8, u32x2{pk2(s, o), pk2(o2, o3)});
    if (tid == 32) st4<SC1>(rw, o_zt + h * 32 + j * 4, pk2(s, o));
  }
  bar();
}

DEV void o_prefetch(rsrc_t rw, u32x4 (&v)[5], int rb, int cb4, u32 o_pq, u32 o_xin) {
  const int tid = otid(), lane = tid & 63, w = tid >> 6;
#pragma unroll
  for (int i = 0; i < 4; ++i) v[i] = ld16<SC1>(rw, o_pq + ((rb * 32 + 4 * w + i) * 64 + lane) * 16);
  v[4] = ld16<SC1>(rw, o_xin + ((rb * 512 + cb4 * 128 + (tid >> 2)) * 32 + (tid & 3) * 8) * 2);
}
template <bool FINAL>
DEV void phase_O(unsigned short* out, rsrc_t rw, char* sm, const u32x4 (&Wo)[16], u32x4 (&v)[5], int rb, int cb4, u32 o_pq, u32 o_kvt, u32 o_zt,
                 u32 o_xin, u32 o_xout, u32 o_st, int sb STAMP_ARG) {
  const int tid = otid(), lane = tid & 63, w = tid >> 6, lq = lane >> 4, r16 = lane & 15;
  const int ecol = tid >> 2, epc = tid & 3;
  const u32 xoff = ((rb * 512 + cb4 * 128 + ecol) * 32 + epc * 8) * 2;
  u32x4 xin;
  {
    u32x2 kv[4], zt[4];
    if (GO) {
      if (!WO1 || tid < 64) {
        kv[3] = ld8<SC1>(rw, o_kvt + ((4 * w + 3) * 64 + lane) * 8);
        for (int spin = 0; spin < SPIN_CAP; ++spin) {
          if (!anyb(isbad(kv[3]))) break;
          __builtin_amdgcn_s_sleep(SLP);
          kv[3] = ld8<SC1>(rw, o_kvt + ((4 * w + 3) * 64 + lane) * 8);
        }
      }
      if (WO1) asm volatile("s_barrier" ::: "memory");
    }
#pragma unroll
    for (int i = 0; i < 4; ++i) { if (!GO || WO1 || i < 3) kv[i] = ld8<SC1>(rw, o_kvt + ((4 * w + i) * 64 + lane) * 8); zt[i] = ld8<SC1>(rw, o_zt + (4 * w + i) * 32 + lq * 8); }
    repoll16<5>(rw, [&](int i) -> u32 { return i < 4 ? o_pq + ((rb * 32 + 4 * w + i) * 64 + lane) * 16 : o_xin + xoff; }, v);
    xin = v[4];
    for (int spin = 0; spin < SPIN_CAP; ++spin) {
      bool bad = false;
#pragma unroll
      for (int i = 0; i < 4; ++i) bad = bad || isbad(kv[i]) || isbad(zt[i]);
      if (!anyb(bad)) break;
      __builtin_amdgcn_s_sleep(SLP);
#pragma unroll
      for (int i = 0; i < 4; ++i) { kv[i] = ld8<SC1>(rw, o_kvt + ((4 * w + i) * 64 + lane) * 8); zt[i] = ld8<SC1>(rw, o_zt + (4 * w + i) * 32 + lq * 8); }
    }
    STAMP(sb + 1);
#pragma unroll
    for (int hp = 0; hp < 2; ++hp)
#pragma unroll
      for (int rt = 0; rt < 2; ++rt) {
        u32x4 of;
#pragma unroll
        for (int t = 0; t < 2; ++t) {
          const int i = hp * 2 + t;
          u32x2 B = {v[i][2 * rt], v[i][2 * rt + 1]};
          f32x4 z4 = {0.f, 0.f, 0.f, 0.f};
          f32x4 num = mfma16(kv[i], B, z4), den = mfma16(zt[i], B, z4);
          const float dr = __builtin_amdgcn_rcpf(rbf(rbf(den[0]) + 1e-6f));
          f32x2 o0 = rbf2(lo2(num)) * dr, o1 = rbf2(hi2(num)) * dr;
          of[2 * t] = pk2(o0[0], o0[1]); of[2 * t + 1] = pk2(o1[0], o1[1]);
        }
        lds_st16(sm, O_OF + ((rt * 16 + 2 * w + hp) * 64 + lane) * 16, of);
      }
  }
  bar();
  {
    f32x4 a0 = {0.f, 0.f, 0.f, 0.f}, a1 = a0;
    u32x4 o0[16], o1[16];
#pragma unroll
    for (int ks = 0; ks < 4; ++ks) { o0[ks] = lds_ld16(sm, O_OF + (ks * 64 + lane) * 16); o1[ks] = lds_ld16(sm, O_OF + ((16 + ks) * 64 + lane) * 16); }
#pragma unroll
    for (int ks = 0; ks < 16; ++ks) {
      if (!FINAL && ks == 2) fill16(rw, O_PQ + XSZ + (u32)(blockIdx.x * NT + tid) * 16);
      if (ks + 4 < 16) { o0[ks + 4] = lds_ld16(sm, O_OF + ((ks + 4) * 64 + lane) * 16); o1[ks + 4] = lds_ld16(sm, O_OF + ((20 + ks) * 64 + lane) * 16); }
      sfence();
      a0 = mfma32(o0[ks], Wo[ks], a0);
      a1 = mfma32(o1[ks], Wo[ks], a1);
      sfence();
    }
    lds_st16(sm, O_T + (16 * w + r16) * 144 + (4 * lq) * 4, __builtin_bit_cast(u32x4, a0));
    lds_st16(sm, O_T + (16 * w + r16) * 144 + (16 + 4 * lq) * 4, __builtin_bit_cast(u32x4, a1));
  }
  bar();
  STAMP(sb + 2);
  f32x4 t0 = __builtin_bit_cast(f32x4, lds_ld16(sm, O_T + ecol * 144 + epc * 32)), t1 = __builtin_bit_cast(f32x4, lds_ld16(sm, O_T + ecol * 144 + epc * 32 + 16));
  u32x4 ov;
#pragma unroll
  for (int j = 0; j < 4; ++j) {
    f32x2 r = unpk(xin[j]) + rbf2(j == 0 ? lo2(t0) : j == 1 ? hi2(t0) : j == 2 ? lo2(t1) : hi2(t1));
    ov[j] = pk2(r[0], r[1]);
  }
  if (!FINAL) {
    st16<SC1>(rw, o_xout + xoff, ov);
#pragma unroll
    for (int j = 0; j < 8; ++j)
      *(LAS unsigned short*)((LAS char*)sm + O_OF + (epc * 8 + j) * 272 + ecol * 2) = (unsigned short)((j & 1) ? (ov[j >> 1] >> 16) : (ov[j >> 1] & 0xffffu));
    bar();
    const int r = tid >> 4, pc = tid & 15;
    u32x4 v = lds_ld16(sm, O_OF + r * 272 + pc * 16);
    float s = 0.f, q = 0.f;
#pragma unroll
    for (int j = 0; j < 4; ++j) { s = dot2(v[j], 0x3f803f80u, s); q = dot2(v[j], v[j], q); }
#pragma unroll
    for (int o = 1; o < 16; o <<= 1) { s += __shfl_xor(s, o); q += __shfl_xor(q, o); }
    float s1 = __shfl_down(s, 16), q1 = __shfl_down(q, 16);
    if ((lane & 31) == 0) st16<SC1>(rw, o_st + (cb4 * L + rb * 32 + r) * 8, u32x4{u_(s), u_(q), u_(s1), u_(q1)});
    bar();
  } else {
#pragma unroll
    for (int j = 0; j < 8; ++j)
      *(LAS unsigned short*)((LAS char*)sm + O_OF + (epc * 8 + j) * 272 + ecol * 2) = (unsigned short)((j & 1) ? (ov[j >> 1] >> 16) : (ov[j >> 1] & 0xffffu));
    bar();
    const int r = tid >> 4, pc = tid & 15;
    *(u32x4*)(out + (size_t)(rb * 32 + r) * 512 + cb4 * 128 + pc * 8) = lds_ld16(sm, O_OF + r * 272 + pc * 16);
  }
}

extern __shared__ __attribute__((aligned(16))) char smem[];
__global__ void __launch_bounds__(512, 2) kB(Params p) {
  STAMP_WS(p); STAMP(0);
  rsrc_t rw = mk_rsrc(p.ws, O_END);
  const int b = blockIdx.x, tid = otid();
  const int d0 = 2 * ((b & 7) * 32 + (b >> 3));
  const int rbq = (b & 7) * 4 + ((b >> 3) & 3), cb = b >> 5;
  const int rbo = (b & 7) * 8 + ((b >> 3) & 7), cb4 = b >> 6;
  const float dsk = bf2f(p.Dskip[d0 + (tid >> 8)]);
  u32x4 W[8][3], Wo[16], ov5[5], xv0, s0, s1;
  phase_A(p, rw, smem, d0, xv0, s0, s1 STAMP_PASS);
  STAMP(2);
  phase_C<1, false>(rw, smem, xv0, s0, s1, W, cb, d0, dsk, O_X0, O_ST0, O_X1, 3 STAMP_PASS);
  q_colsum(smem, W);
  STAMP(6);
  phase_Q<true>(rw, smem, W, Wo, rbq, cb, cb4, O_X1, O_PQ, O_KVP, O_ZP, 6 STAMP_PASS);
  STAMP(9);
  if (PF) o_prefetch(rw, ov5, rbo, cb4, O_PQ, O_X1);
  phase_R(rw, smem, b, O_KVP, O_ZP, O_KVT, O_ZT);
  if (!PF) o_prefetch(rw, ov5, rbo, cb4, O_PQ, O_X1);
  STAMP(10);
  phase_O<false>(p.out, rw, smem, Wo, ov5, rbo, cb4, O_PQ, O_KVT, O_ZT, O_X1, O_X2, O_ST2, 10 STAMP_PASS);
  STAMP(13);
  phase_C<4, true>(rw, smem, xv0, s0, s1, W, cb, d0, dsk, O_X2, O_ST2, O_X3, 13 STAMP_PASS);
  STAMP(16);
  phase_Q<false>(rw, smem, W, Wo, rbq, cb, cb4, O_X3, O_PQ + XSZ, O_KVP + KVP_SZ, O_ZP + ZP_SZ, 16 STAMP_PASS);
  STAMP(19);
  if (PF) o_prefetch(rw, ov5, rbo, cb4, O_PQ + XSZ, O_X3);
  phase_R(rw, smem, b, O_KVP + KVP_SZ, O_ZP + ZP_SZ, O_KVT + KVT_SZ, O_ZT + ZT_SZ);
  if (!PF) o_prefetch(rw, ov5, rbo, cb4, O_PQ + XSZ, O_X3);
  STAMP(20);
  phase_O<true>(p.out, rw, smem, Wo, ov5, rbo, cb4, O_PQ + XSZ, O_KVT + KVT_SZ, O_ZT + ZT_SZ, O_X3, 0u, 0u, 20 STAMP_PASS);
  STAMP(23);
}
static Params make_params(void* const* d_in, void* d_out, void* d_ws) {
  Params p;
  p.u = (const u32*)d_in[0]; p.logA = (const unsigned short*)d_in[1]; p.Aim = (const unsigned short*)d_in[2];
  p.Cre = (const unsigned short*)d_in[3]; p.Cim = (const unsigned short*)d_in[4]; p.logdt = (const unsigned short*)d_in[5];
  p.Dskip = (const unsigned short*)d_in[6]; p.wqkv = (const unsigned short*)d_in[8]; p.wo = (const unsigned short*)d_in[9];
  p.ws = (char*)d_ws; p.out = (unsigned short*)d_out;
  return p;
}
extern "C" void kernel_launch(void* const* d_in, const int* in_sizes, int n_in, void* d_out, int out_size, void* d_ws, size_t ws_size,
                              hipStream_t stream) {
  Params p = make_params(d_in, d_out, d_ws);
  (void)hipFuncSetAttribute((const void*)kB, hipFuncAttributeMaxDynamicSharedMemorySize, P_LDS);
  kB<<<NB, NT, P_LDS, stream>>>(p);
}
```

```cpp
#include <hip/hip_runtime.h>
#include <hip/hip_bf16.h>
#include <stdint.h>

typedef unsigned u32;
typedef unsigned u32x4 __attribute__((ext_vector_type(4)));
typedef unsigned u32x2 __attribute__((ext_vector_type(2)));
typedef float f32x4 __attribute__((ext_vector_type(4)));
typedef float f32x2 __attribute__((ext_vector_type(2)));
typedef short s16x4 __attribute__((ext_vector_type(4)));
typedef __bf16 bf16x8 __attribute__((ext_vector_type(8)));
typedef __bf16 bf16x2 __attribute__((ext_vector_type(2)));
#define LAS __attribute__((address_space(3)))
#define DEV __device__ __forceinline__

constexpr int L = 2048, D = 512, NB = 256, NT = 512;
constexpr u32 O_WQ = 0;
constexpr u32 O_WO = O_WQ + 1536 * 512 * 2;
constexpr u32 O_KR = O_WO + 512 * 512 * 2;
constexpr u32 KR_STR = 2064 * 2;
constexpr u32 XSZ = L * D * 2;
constexpr u32 O_X0 = O_KR + 512 * KR_STR;
constexpr u32 O_ST0 = O_X0 + XSZ;
constexpr u32 O_X1 = O_ST0 + L * 8;
constexpr u32 O_X2 = O_X1 + XSZ;
constexpr u32 O_X3 = O_X2 + XSZ;
constexpr u32 O_ST2 = O_X3 + XSZ;
constexpr u32 O_PQ = O_ST2 + 4 * L * 8;
constexpr u32 O_KVP = O_PQ + 2 * XSZ;
constexpr u32 KVP_SZ = 32 * 32 * 1024;
constexpr u32 O_ZP = O_KVP + 2 * KVP_SZ;
constexpr u32 ZP_SZ = 32 * 32 * 64;
constexpr u32 O_KVT = O_ZP + 2 * ZP_SZ;
constexpr u32 KVT_SZ = 32 * 512;
constexpr u32 O_ZT = O_KVT + 2 * KVT_SZ;
constexpr u32 ZT_SZ = 32 * 32;
constexpr u32 FLG_STR = 64;
constexpr u32 O_FLG = O_ZT + 2 * ZT_SZ;
constexpr u32 O_EPO = O_FLG + 256 * FLG_STR;
constexpr u32 O_END = O_EPO + 128;

struct Params {
  const u32* u; const unsigned short* logA; const unsigned short* Aim; const unsigned short* Cre; const unsigned short* Cim;
  const unsigned short* logdt; const unsigned short* Dskip; const unsigned short* wqkv; const unsigned short* wo;
  char* ws; unsigned short* out;
};

DEV float bf2f(u32 h) { return __builtin_bit_cast(float, h << 16); }
DEV float bflo(u32 w) { return __builtin_bit_cast(float, w << 16); }
DEV float bfhi(u32 w) { return __builtin_bit_cast(float, w & 0xffff0000u); }
DEV u32 pk2(float lo, float hi) { f32x2 f = {lo, hi}; return __builtin_bit_cast(u32, __builtin_convertvector(f, bf16x2)); }
DEV float rbf(float x) { return bflo(pk2(x, x)); }
DEV f32x4 mfma32(u32x4 a, u32x4 b, f32x4 c) {
  return __builtin_amdgcn_mfma_f32_16x16x32_bf16(__builtin_bit_cast(bf16x8, a), __builtin_bit_cast(bf16x8, b), c, 0, 0, 0);
}
DEV f32x4 mfma16(u32x2 a, u32x2 b, f32x4 c) {
  return __builtin_amdgcn_mfma_f32_16x16x16bf16_1k(__builtin_bit_cast(s16x4, a), __builtin_bit_cast(s16x4, b), c, 0, 0, 0);
}
DEV float dot2(u32 a, u32 b, float c) { return __builtin_amdgcn_fdot2_f32_bf16(__builtin_bit_cast(bf16x2, a), __builtin_bit_cast(bf16x2, b), c, false); }
typedef __amdgpu_buffer_rsrc_t rsrc_t;
DEV rsrc_t mk_rsrc(const void* p, u32 bytes) { return __builtin_amdgcn_make_buffer_rsrc((void*)p, (short)0, (int)bytes, 0x00020000); }
template <int AUX> DEV u32x4 ld16(rsrc_t r, u32 off) { return __builtin_amdgcn_raw_buffer_load_b128(r, off, 0, AUX); }
template <int AUX> DEV u32x2 ld8(rsrc_t r, u32 off) { return __builtin_amdgcn_raw_buffer_load_b64(r, off, 0, AUX); }
template <int AUX> DEV u32 ld4(rsrc_t r, u32 off) { return __builtin_amdgcn_raw_buffer_load_b32(r, off, 0, AUX); }
template <int AUX> DEV void st16(rsrc_t r, u32 off, u32x4 v) { __builtin_amdgcn_raw_buffer_store_b128(v, r, off, 0, AUX); }
template <int AUX> DEV void st8(rsrc_t r, u32 off, u32x2 v) { __builtin_amdgcn_raw_buffer_store_b64(v, r, off, 0, AUX); }
template <int AUX> DEV void st4(rsrc_t r, u32 off, u32 v) { __builtin_amdgcn_raw_buffer_store_b32(v, r, off, 0, AUX); }
DEV u32x4 lds_ld16(const char* s, u32 off) { return *(const LAS u32x4*)((const LAS char*)s + off); }
DEV u32x2 lds_ld8(const char* s, u32 off) { return *(const LAS u32x2*)((const LAS char*)s + off); }
DEV u32 lds_ld4(const char* s, u32 off) { return *(const LAS u32*)((const LAS char*)s + off); }
DEV void lds_st16(char* s, u32 off, u32x4 v) { *(LAS u32x4*)((LAS char*)s + off) = v; }
DEV void lds_st8(char* s, u32 off, u32x2 v) { *(LAS u32x2*)((LAS char*)s + off) = v; }
DEV void lds_st4(char* s, u32 off, u32 v) { *(LAS u32*)((LAS char*)s + off) = v; }
DEV u32x2 lds_tr(const char* s, u32 off) {
  return __builtin_bit_cast(u32x2, __builtin_amdgcn_ds_read_tr16_b64_v4i16((LAS s16x4*)((LAS char*)s + off)));
}
DEV float f_(u32 x) { return __builtin_bit_cast(float, x); }
DEV u32 u_(float x) { return __builtin_bit_cast(u32, x); }
template <int CTRL> DEV float dpp_add(float v) {
  return v + __builtin_bit_cast(float, __builtin_amdgcn_update_dpp(0, __builtin_bit_cast(int, v), CTRL, 0xf, 0xf, true));
}
DEV float row_sum16(float v) { v = dpp_add<0xB1>(v); v = dpp_add<0x4E>(v); v = dpp_add<0x141>(v); return dpp_add<0x140>(v); }
DEV float lane_of(float v, int l) { return __builtin_bit_cast(float, __builtin_amdgcn_readlane(__builtin_bit_cast(int, v), l)); }
DEV float xor16(float v) { return __shfl_xor(v, 16); }
DEV float xor32(float v) { return __shfl_xor(v, 32); }
DEV void bar() { asm volatile("s_waitcnt lgkmcnt(0)\n\ts_barrier" ::: "memory"); }
DEV int otid() { int t = threadIdx.x; asm volatile("" : "+v"(t)); return t; }
DEV void sfence() { __builtin_amdgcn_sched_barrier(0); }
constexpr u32 SENT = 0xffffffffu;
constexpr int SC1 = 16;
#define GC 8
#define GQ 0
#define GO 1
#define WC 0
#define WQ1 0
#define WO1 0
#define RG 0
#define OG 0
#define SLP 1
constexpr int SPIN_CAP = 1 << 14;
DEV bool isbad(u32x4 v) { return max(max(v[0], v[1]), max(v[2], v[3])) == SENT; }
DEV bool isbad(u32x2 v) { return max(v[0], v[1]) == SENT; }
DEV bool anyb(bool b) { return __builtin_amdgcn_ballot_w64(b) != 0ull; }
template <int N, class F> DEV void repoll16(rsrc_t r, F off, u32x4 (&v)[N]) {
  for (int spin = 0; spin < SPIN_CAP; ++spin) {
    u32 mask = 0;
#pragma unroll
    for (int i = 0; i < N; ++i) mask |= anyb(isbad(v[i])) ? (1u << i) : 0u;
    if (!mask) break;
    __builtin_amdgcn_s_sleep(SLP);
#pragma unroll
    for (int i = 0; i < N; ++i) if (mask & (1u << i)) v[i] = ld16<SC1>(r, off(i));
  }
}
template <int N, int G, bool OPT, class F> DEV void poll16(rsrc_t r, F off, u32x4 (&v)[N]) {
  constexpr int g = G >= 0 ? G : 0;
  if (OPT) {
#pragma unroll
    for (int i = 0; i < N; ++i) v[i] = ld16<SC1>(r, off(i));
  } else if (G >= 0) v[g] = ld16<SC1>(r, off(g));
  if (G >= 0) {
    for (int spin = 0; spin < SPIN_CAP; ++spin) {
      if (!anyb(isbad(v[g]))) break;
      __builtin_amdgcn_s_sleep(SLP);
      v[g] = ld16<SC1>(r, off(g));
    }
  }
  if (!OPT) {
#pragma unroll
    for (int i = 0; i < N; ++i) if (i != G) v[i] = ld16<SC1>(r, off(i));
  }
  repoll16<N>(r, off, v);
}
DEV int permk(int g, int j) { return j < 4 ? 4 * g + j : 16 + 4 * g + (j - 4); }
DEV void ln_stats(float sum, float sq, float& m, float& rinv) {
  float mean = sum * (1.0f / 512.0f);
  float var = fmaxf(sq * (1.0f / 512.0f) - mean * mean, 0.0f);
  m = rbf(mean);
  float den = rbf(__builtin_amdgcn_sqrtf(rbf(rbf(var) + 1e-5f)));
  rinv = __builtin_amdgcn_rcpf(den);
}
DEV f32x2 unpk(u32 w) { return f32x2{bflo(w), bfhi(w)}; }
DEV f32x2 rbf2(f32x2 v) { return unpk(pk2(v[0], v[1])); }
DEV f32x2 lo2(f32x4 v) { return f32x2{v[0], v[1]}; }
DEV f32x2 hi2(f32x4 v) { return f32x2{v[2], v[3]}; }
DEV u32 phi2(f32x2 a) {
  f32x2 t = rbf2(a), s = t * 1.4426950408889634f, p = t + 1.0f;
  f32x2 e = {__builtin_amdgcn_exp2f(s[0]), __builtin_amdgcn_exp2f(s[1])};
  f32x2 q = {t[0] > 0.0f ? p[0] : e[0], t[1] > 0.0f ? p[1] : e[1]};
  q = rbf2(q) + 1e-6f;
  return pk2(q[0], q[1]);
}
DEV float phi(float t) { float p = t > 0.0f ? t + 1.0f : __expf(t); return rbf(rbf(p) + 1e-6f); }
#define STAMP(i)
#define STAMP_WS(p)
#define STAMP_ARG
#define STAMP_PASS
constexpr u32 KA_ROW = 528;
constexpr u32 KA_TAB = 16 * KA_ROW;
constexpr u32 KA_G = 0, KA_E = 8 * KA_TAB;
constexpr u32 KA_KC = 16 * KA_TAB;
constexpr u32 KA_XT = KA_KC + 2 * 656 * 4;
constexpr u32 KA_LDS = KA_XT + 8 * 1040;
constexpr u32 KA_WIN = 0, KA_WSTR = 2064 * 16;
constexpr u32 KA_R = 100352;
#define KST 16
typedef f32x2 cf;
DEV cf cmul(cf a, cf b) { return f32x2{a[0], a[0]} * b + f32x2{a[1], a[1]} * f32x2{-b[1], b[0]}; }
DEV cf cexp_pow(float are, float aim, int k) {
  float s = (float)(1 << k);
  float mag = __expf(are * s);
  double rev = (double)aim * (0.15915494309189535 * (double)(1 << k));
  float fr = (float)(rev - __builtin_floor(rev));
  return f32x2{__builtin_amdgcn_cosf(fr), __builtin_amdgcn_sinf(fr)} * mag;
}
DEV void phase_A(const Params& p, rsrc_t rw, char* sm, int d0, u32x4& xv0, u32x4& s0, u32x4& s1 STAMP_ARG) {
  const int tid = otid(), b = blockIdx.x, lane = tid & 63, w = tid >> 6;
  const int n = tid & 127, ch = (tid >> 7) & 1, sih = tid >> 8;
  const int d = d0 + ch;
  const u32 rlA = p.logA[d * 128 + n], rAi = p.Aim[d * 128 + n], rCr = p.Cre[d * 128 + n], rCi = p.Cim[d * 128 + n], rdt = p.logdt[d];
  const u32 tag = ld4<SC1>(rw, O_EPO) + 1u;
  sfence();
  const int rg = (b & 7) * 32 + (b >> 3);
  const int row = rg * 8 + w;
  u32x4 xv = *(const u32x4*)(p.u + (size_t)row * 256 + lane * 4);
  u32 wv[8]; u32 wdst;
  {
    int g = b * NT + tid, ln = g & 63, lq = ln >> 4;
    const unsigned short* src; int ld, ks;
    if (g < 98304) {
      int f = g >> 6, ty = f % 3, f2 = f / 3, ks8 = f2 & 7, kh = (f2 >> 3) & 1, hh = (f2 >> 4) & 3, cb = f2 >> 6;
      ks = kh * 8 + ks8; src = p.wqkv + ty * 512 + (4 * cb + hh) * 16 + (ln & 15); ld = 1536; wdst = O_WQ + (u32)g * 16;
    } else {
      int g2 = g - 98304, f = g2 >> 6; ks = f & 15; int ww = (f >> 4) & 7, cb4 = f >> 7;
      src = p.wo + 128 * cb4 + 16 * ww + (ln & 15); ld = 512; wdst = O_WO + (u32)g2 * 16;
    }
#pragma unroll
    for (int j = 0; j < 8; ++j) wv[j] = src[(size_t)(32 * ks + permk(lq, j)) * ld];
  }
  {
    const u32 g = (u32)(b * NT + tid) * 16;
    const u32x4 sv = {SENT, SENT, SENT, SENT};
    st16<KST>(rw, O_X1 + g, sv);
    if (g < O_FLG - O_ZP) st16<KST>(rw, O_ZP + g, sv);
    if (g < 4 * L * 8) st16<KST>(rw, O_ST2 + g, sv);
  }
  sfence();
  const float lA = bf2f(rlA), Ai = bf2f(rAi), Cr = bf2f(rCr), Ci = bf2f(rCi), dt = rbf(expf(bf2f(rdt)));
  float are = -rbf(expf(lA)) * dt, aim = Ai * dt;
  cf C = {Cr * dt, Ci * dt};
#pragma unroll
  for (int t = 0; t < 2; ++t) {
    const int si = sih + 2 * t;
    const u32 tb = (u32)(ch * 4 + si) * KA_TAB + n * 4;
    cf v0 = cexp_pow(are, aim, si), v1 = cmul(v0, v0), v2 = cmul(v1, v1), v3 = cmul(v2, v2);
    cf e[8]; e[0] = f32x2{1.f, 0.f}; e[1] = v0; e[2] = v1; e[3] = cmul(v1, v0); e[4] = v2;
    e[5] = cmul(v2, v0); e[6] = cmul(v2, v1); e[7] = cmul(v2, e[3]);
#pragma unroll
    for (int j = 0; j < 8; ++j) {
      lds_st4(sm, KA_E + tb + j * KA_ROW, pk2(e[j][0], e[j][1]));
      cf f = cmul(e[j], v3);
      lds_st4(sm, KA_E + tb + (j + 8) * KA_ROW, pk2(f[0], f[1]));
    }
    cf g0 = cexp_pow(are, aim, 4 + si), g1 = cmul(g0, g0), g2 = cmul(g1, g1), g3 = cmul(g2, g2);
    cf h[8]; h[0] = (si == 0) ? C : cmul(C, g3);
    h[1] = cmul(h[0], g0); h[2] = cmul(h[0], g1); h[3] = cmul(h[1], g1);
#pragma unroll
    for (int j = 0; j < 4; ++j) h[4 + j] = cmul(h[j], g2);
#pragma unroll
    for (int j = 0; j < 8; ++j) {
      lds_st4(sm, KA_G + tb + j * KA_ROW, pk2(h[j][0], -h[j][1]));
      cf f = cmul(h[j], g3);
      lds_st4(sm, KA_G + tb + (j + 8) * KA_ROW, pk2(f[0], -f[1]));
    }
  }
  sfence();
  STAMP(24);
  {
    float s = 0.f, q = 0.f;
#pragma unroll
    for (int j = 0; j < 4; ++j) { s = dot2(xv[j], 0x3f803f80u, s); q = dot2(xv[j], xv[j], q); }
    s = row_sum16(s); q = row_sum16(q);
    s = (lane_of(s, 0) + lane_of(s, 16)) + (lane_of(s, 32) + lane_of(s, 48));
    q = (lane_of(q, 0) + lane_of(q, 16)) + (lane_of(q, 32) + lane_of(q, 48));
    if (lane == 0) { u32x2 v = {u_(s), u_(q)}; st8<KST>(rw, O_ST0 + row * 8, v); }
    lds_st16(sm, KA_XT + w * 1040 + lane * 16, xv);
  }
  { u32x4 v = {wv[0] | (wv[1] << 16), wv[2] | (wv[3] << 16), wv[4] | (wv[5] << 16), wv[6] | (wv[7] << 16)}; st16<KST>(rw, wdst, v); }
  bar();
  {
    u32 v[8];
#pragma unroll
    for (int r = 0; r < 8; ++r) v[r] = *(const LAS unsigned short*)((LAS char*)sm + KA_XT + r * 1040 + tid * 2);
    u32x4 o = {v[0] | (v[1] << 16), v[2] | (v[3] << 16), v[4] | (v[5] << 16), v[6] | (v[7] << 16)};
    st16<KST>(rw, O_X0 + (((rg >> 2) * 512 + tid) * 32 + (rg & 3) * 8) * 2, o);
  }
  sfence();
  STAMP(25);
  {
    const int wc = w >> 2, si = w & 3, lq = lane >> 4, r16 = lane & 15;
    const u32 base = (u32)(wc * 4 + si) * KA_TAB + r16 * KA_ROW + lq * 16;
    f32x4 acc = {0.f, 0.f, 0.f, 0.f};
#pragma unroll
    for (int ks = 0; ks < 8; ++ks) acc = mfma32(lds_ld16(sm, KA_G + base + ks * 64), lds_ld16(sm, KA_E + base + ks * 64), acc);
#pragma unroll
    for (int r = 0; r < 4; ++r) {
      int i = 4 * lq + r;
      int idx = si == 0 ? 16 * i + r16 : 256 + (si - 1) * 128 + 16 * i + r16;
      bool ok = si == 0 || i < 8 || (si == 3 && i == 8 && r16 == 0);
      if (ok) lds_st4(sm, KA_KC + (wc * 656 + idx) * 4, u_(2.0f * acc[r]));
    }
  }
  bar();
  const int c2 = tid >> 8, m = tid & 255;
  {
    u32 o[4];
#pragma unroll
    for (int j = 0; j < 4; ++j) {
      float kv[2];
#pragma unroll
      for (int h = 0; h < 2; ++h) {
        int l = 2047 - (8 * m + 2 * j + h);
        int e = l < 256 ? 0 : (l < 512 ? 1 : (l < 1024 ? 2 : 3));
        int mm = (l + ((1 << e) >> 1) - 1 + ((l >> e) & 1)) >> e;
        int idx = e == 0 ? l : 256 + (e - 1) * 128 + (mm - 128);
        kv[h] = f_(lds_ld4(sm, KA_KC + (c2 * 656 + idx) * 4));
      }
      o[j] = pk2(kv[0], kv[1]);
    }
    lds_st16(sm, KA_R + c2 * KR_STR + m * 16, u32x4{o[0], o[1], o[2], o[3]});
    if (m < 2) lds_st16(sm, KA_R + c2 * KR_STR + 4096 + m * 16, u32x4{0u, 0u, 0u, 0u});
  }
  asm volatile("s_waitcnt vmcnt(0)" ::: "memory");
  bar();
  if (tid == 0) st4<SC1>(rw, O_FLG + b * FLG_STR, tag);
  STAMP(26);
#pragma unroll
  for (int i = 0; i < 4; ++i) {
    const u32 rb_ = KA_R + c2 * KR_STR + (m + 256 * i) * 4;
    u32 a[5];
#pragma unroll
    for (int j = 0; j < 5; ++j) a[j] = lds_ld4(sm, rb_ + j * 4);
    const u32 gb = KA_WIN + c2 * KA_WSTR + (m + 256 * i) * 32;
    lds_st16(sm, gb, u32x4{a[0], a[1], a[2], a[3]});
    lds_st16(sm, gb + 16, u32x4{__builtin_amdgcn_alignbit(a[1], a[0], 16), __builtin_amdgcn_alignbit(a[2], a[1], 16),
                                __builtin_amdgcn_alignbit(a[3], a[2], 16), __builtin_amdgcn_alignbit(a[4], a[3], 16)});
  }
  if (m < 16) lds_st16(sm, KA_WIN + c2 * KA_WSTR + (2048 + m) * 16, u32x4{0u, 0u, 0u, 0u});
  if (w < 4) {
    u32 fl = ld4<SC1>(rw, O_FLG + tid * FLG_STR);
    for (int spin = 0; spin < SPIN_CAP; ++spin) {
      if (!anyb(fl != tag)) break;
      __builtin_amdgcn_s_sleep(SLP);
      fl = ld4<SC1>(rw, O_FLG + tid * FLG_STR);
    }
  }
  bar();
  if (b == 0 && tid == 0) st4<SC1>(rw, O_EPO, tag);
  STAMP(27);
  xv0 = ld16<SC1>(rw, O_X0 + (((m >> 2) * 512 + d0 + c2) * 32 + (m & 3) * 8) * 2);
  s0 = ld16<SC1>(rw, O_ST0 + tid * 32); s1 = ld16<SC1>(rw, O_ST0 + tid * 32 + 16);
}
#define PF 0
constexpr u32 P_G = 0, P_GSTR = 2064 * 16;
constexpr u32 DB = 2 * P_GSTR;
constexpr u32 P_CS = 158720;
constexpr u32 P_LDS = P_CS + 8 * 3 * 16 * 4;
constexpr u32 C_U = DB, C_ST = C_U + 2 * 4608, C_RED = C_ST + L * 8;
constexpr u32 Q_A = DB, Q_EX = DB, Q_SP = DB + 65536, Q_MR = Q_SP + 4096;
constexpr u32 O_OF = DB, O_T = DB + 32768, O_PS = O_T + 128 * 144;
constexpr u32 R_B = DB;
static_assert(KA_LDS <= P_CS && KA_WIN == P_G && KA_WSTR == P_GSTR && KA_R + 2 * KR_STR <= KA_KC && KA_R >= C_RED && C_RED + 65536 <= P_CS && Q_MR + 8 * 512 <= P_CS && P_LDS <= 163840, "LDS map");

DEV void fill16(rsrc_t rw, u32 off) { st16<SC1>(rw, off, u32x4{SENT, SENT, SENT, SENT}); }
template <int NP, bool POLL>
DEV void phase_C(rsrc_t rw, char* sm, u32x4 xv0, u32x4 s0, u32x4 s1, u32x4 (&W)[8][3], int cb, int d0, float dsk, u32 o_in, u32 o_st, u32 o_out, int sb STAMP_ARG) {
  const int tid = otid(), lane = tid & 63, w = tid >> 6, ch = tid >> 8, m = tid & 255;
  const u32 xoff = (((m >> 2) * 512 + d0 + ch) * 32 + (m & 3) * 8) * 2;
  u32x4 xv;
  {
    u32x4 sv[2 * NP + 1];
    auto off = [&](int i) -> u32 { return i == 2 * NP ? o_in + xoff : o_st + (i >> 1) * L * 8 + tid * 32 + (i & 1) * 16; };
    if (POLL) poll16<2 * NP + 1, (POLL ? GC : -1), WC>(rw, off, sv);
    else {
      sv[0] = s0; sv[1] = s1; sv[2 * NP] = xv0;
      const u32 fg = (u32)(blockIdx.x * NT + tid) * 16;
      fill16(rw, O_PQ + fg); fill16(rw, O_KVP + fg);
    }
    xv = sv[2 * NP];
    float mr[8];
#pragma unroll
    for (int r = 0; r < 4; ++r) {
      float s = 0.f, q2 = 0.f;
#pragma unroll
      for (int q = 0; q < NP; ++q) { s += f_(sv[2 * q + (r >> 1)][(r & 1) * 2]); q2 += f_(sv[2 * q + (r >> 1)][(r & 1) * 2 + 1]); }
      ln_stats(s, q2, mr[2 * r], mr[2 * r + 1]);
    }
    lds_st16(sm, C_ST + tid * 32, u32x4{u_(mr[0]), u_(mr[2]), u_(mr[1]), u_(mr[3])});
    lds_st16(sm, C_ST + tid * 32 + 16, u32x4{u_(mr[4]), u_(mr[6]), u_(mr[5]), u_(mr[7])});
    if (tid < 64) lds_st16(sm, C_U + (tid >> 5) * 4608 + ((tid >> 4) & 1) * 2304 + (tid & 15) * 16, u32x4{0u, 0u, 0u, 0u});
  }
  asm volatile("s_waitcnt vmcnt(0)" ::: "memory");
  bar();
  STAMP(sb + 1);
  u32x4 uv;
#pragma unroll
  for (int j = 0; j < 4; ++j) {
    u32x4 s = lds_ld16(sm, C_ST + (8 * m + 2 * j) * 8);
    f32x2 t = rbf2(unpk(xv[j]) - f32x2{f_(s[0]), f_(s[1])}) * f32x2{f_(s[2]), f_(s[3])};
    uv[j] = pk2(t[0], t[1]);
  }
  lds_st16(sm, C_U + ch * 4608 + (m & 1) * 2304 + (16 + (m >> 1)) * 16, uv);
  bar();
  {
    const int wc = w >> 2, w4 = w & 3, lq = lane >> 4, r16 = lane & 15;
    const int j0 = (lq & 1) * 16 + (lq >> 1) * 8, rho = 2 * w4;
    const u32 ub = C_U + wc * 4608 + (lq >> 1) * 2304 + (15 - 2 * rho + r16 + (lq & 1)) * 16;
    const u32 gb = P_G + wc * P_GSTR + (2031 - 32 * (rho + 1) - 256 * 7 - r16 + j0) * 16;
    f32x4 acc[8];
#pragma unroll
    for (int c = 0; c < 8; ++c) acc[c] = f32x4{0.f, 0.f, 0.f, 0.f};
    u32x4 B0[8], A0[8], B1[8], A1[8];
#pragma unroll
    for (int k = 0; k < 8; ++k) { if (k == 0) A0[0] = lds_ld16(sm, gb + 512 + 7 * 4096); B0[7 - k] = lds_ld16(sm, ub + (7 - k) * 256); }
#pragma unroll
    for (int k = 1; k < 8; ++k) A0[k] = lds_ld16(sm, gb + 512 + (7 - k) * 4096);
    sfence();
    const u32 wb = O_WQ + (u32)(cb * 8 + w) * 24576 + lane * 16;
#pragma unroll
    for (int g = 0; g < 8; ++g) {
      A1[g] = lds_ld16(sm, gb + (7 - g) * 4096); B1[g] = lds_ld16(sm, ub - 32 + g * 256);
#pragma unroll
      for (int i = 2 * g; i < 2 * g + 2; ++i) W[i / 3][i % 3] = ld16<SC1>(rw, wb + i * 1024);
      sfence();
#pragma unroll
      for (int c = 7; c >= g; --c) acc[c] = mfma32(A0[g], B0[c - g], acc[c]);
      sfence();
    }
#pragma unroll
    for (int g = 0; g < 8; ++g) {
      W[(16 + g) / 3][(16 + g) % 3] = ld16<SC1>(rw, wb + (16 + g) * 1024);
      sfence();
#pragma unroll
      for (int c = g; c < 8; ++c) acc[c] = mfma32(A1[g], B1[c - g], acc[c]);
      sfence();
    }
#pragma unroll
    for (int c = 0; c < 8; ++c) lds_st16(sm, C_RED + ((wc * 4 + w4) * 8 + c) * 1024 + lane * 16, __builtin_bit_cast(u32x4, acc[c]));
  }
  bar();
  STAMP(sb + 2);
  {
    const int c = m >> 5, a = (m & 31) >> 1, lq0 = (m & 1) * 2;
    f32x4 y0 = {0.f, 0.f, 0.f, 0.f}, y1 = y0;
#pragma unroll
    for (int q = 0; q < 4; ++q) {
      const u32 o = C_RED + ((ch * 4 + q) * 8 + c) * 1024 + (lq0 * 16 + a) * 16;
      y0 += __builtin_bit_cast(f32x4, lds_ld16(sm, o)); y1 += __builtin_bit_cast(f32x4, lds_ld16(sm, o + 256));
    }
    u32x4 ov;
#pragma unroll
    for (int j = 0; j < 4; ++j) {
      f32x2 y = j == 0 ? lo2(y0) : j == 1 ? hi2(y0) : j == 2 ? lo2(y1) : hi2(y1);
      f32x2 r = unpk(xv[j]) + rbf2(rbf2(y) + rbf2(unpk(uv[j]) * dsk));
      ov[j] = pk2(r[0], r[1]);
    }
    st16<SC1>(rw, o_out + xoff, ov);
  }
  bar();
}

DEV void q_colsum(char* sm, const u32x4 (&W)[8][3]) {
  const int tid = otid(), lane = tid & 63, w = tid >> 6;
#pragma unroll
  for (int ty = 0; ty < 3; ++ty) {
    float s = 0.f;
#pragma unroll
    for (int ks = 0; ks < 8; ++ks)
#pragma unroll
      for (int j = 0; j < 4; ++j) s = dot2(W[ks][ty][j], 0x3f803f80u, s);
    s += xor16(s); s += xor32(s);
    if (lane < 16) lds_st4(sm, P_CS + ((w * 3 + ty) * 16 + lane) * 4, u_(s));
  }
}
template <bool FIRST>
DEV void phase_Q(rsrc_t rw, char* sm, const u32x4 (&W)[8][3], u32x4 (&Wo)[16], int rb, int cb, int cb4, u32 o_x, u32 o_pq, u32 o_kvp, u32 o_zp,
                 int sb STAMP_ARG) {
  const int tid = otid(), lane = tid & 63, w = tid >> 6, hh = w >> 1, kh = w & 1, lq = lane >> 4, r16 = lane & 15;
  {
    u32x4 av[8];
    poll16<8, GQ, WQ1>(rw, [&](int i) -> u32 { return o_x + (u32)rb * 65536 + (tid + i * 512) * 16; }, av);
    const int chn = tid >> 2, pc = tid & 3, f = (chn >> 1) & 3;
#pragma unroll
    for (int i = 0; i < 8; ++i)
      lds_st16(sm, Q_A + (chn + 128 * (i & 3)) * 128 + ((((i >> 2) * 2 + (pc >> 1)) ^ f) * 32) + (pc & 1) * 16, av[i]);
  }
  bar();
  STAMP(sb + 1);
  f32x4 acc[4][3];
#pragma unroll
  for (int rt = 0; rt < 4; ++rt)
#pragma unroll
    for (int ty = 0; ty < 3; ++ty) acc[rt][ty] = f32x4{0.f, 0.f, 0.f, 0.f};
  const int chl = 4 * lq + (r16 >> 2), f = (chl >> 1) & 3;
  u32 ab[4];
#pragma unroll
  for (int rt = 0; rt < 4; ++rt) ab[rt] = Q_A + (kh * 256 + chl) * 128 + ((rt ^ f) * 32) + (r16 & 3) * 8;
  const u32 abs_ = Q_A + (kh * 256 + chl) * 128 + ((hh ^ f) * 32) + (r16 & 3) * 8;
  float ssum = 0.f, ssq = 0.f;
  const u32 wob = O_WO + (u32)(cb4 * 8 + w) * 16384 + lane * 16;
  {
    u32x2 xa[2][4][2], xs[2][2];
#pragma unroll
    for (int rt = 0; rt < 4; ++rt) { xa[0][rt][0] = lds_tr(sm, ab[rt]); xa[0][rt][1] = lds_tr(sm, ab[rt] + 2048); }
    xs[0][0] = lds_tr(sm, abs_); xs[0][1] = lds_tr(sm, abs_ + 2048);
#pragma unroll
    for (int ks = 0; ks < 8; ++ks) {
      const int cu = ks & 1, nx = cu ^ 1;
      if (ks < 7) {
#pragma unroll
        for (int rt = 0; rt < 4; ++rt) { xa[nx][rt][0] = lds_tr(sm, ab[rt] + (ks + 1) * 4096); xa[nx][rt][1] = lds_tr(sm, ab[rt] + (ks + 1) * 4096 + 2048); }
        xs[nx][0] = lds_tr(sm, abs_ + (ks + 1) * 4096); xs[nx][1] = lds_tr(sm, abs_ + (ks + 1) * 4096 + 2048);
      }
      Wo[2 * ks] = ld16<SC1>(rw, wob + 2 * ks * 1024); Wo[2 * ks + 1] = ld16<SC1>(rw, wob + (2 * ks + 1) * 1024);
      if (FIRST && ks == 1) fill16(rw, O_X2 + (u32)(blockIdx.x * NT + tid) * 16);
      if (FIRST && ks == 4) fill16(rw, O_X3 + (u32)(blockIdx.x * NT + tid) * 16);
      sfence();
#pragma unroll
      for (int rt = 0; rt < 4; ++rt) {
        u32x4 X = {xa[cu][rt][0][0], xa[cu][rt][0][1], xa[cu][rt][1][0], xa[cu][rt][1][1]};
        acc[rt][0] = mfma32(W[ks][0], X, acc[rt][0]);
        acc[rt][1] = mfma32(X, W[ks][1], acc[rt][1]);
        acc[rt][2] = mfma32(X, W[ks][2], acc[rt][2]);
      }
#pragma unroll
      for (int j = 0; j < 2; ++j)
#pragma unroll
        for (int e = 0; e < 2; ++e) { ssum = dot2(xs[cu][j][e], 0x3f803f80u, ssum); ssq = dot2(xs[cu][j][e], xs[cu][j][e], ssq); }
      sfence();
    }
  }
  lds_st8(sm, Q_SP + ((kh * 4 + lq) * 64 + hh * 16 + r16) * 8, u32x2{u_(ssum), u_(ssq)});
  bar();
  const u32 ex = Q_EX + hh * 12288 + lane * 16;
  if (kh == 0) {
#pragma unroll
    for (int rt = 0; rt < 4; ++rt) lds_st16(sm, ex + rt * 1024, __builtin_bit_cast(u32x4, acc[rt][0]));
  } else {
#pragma unroll
    for (int rt = 0; rt < 4; ++rt) {
      lds_st16(sm, ex + (4 + rt * 2) * 1024, __builtin_bit_cast(u32x4, acc[rt][1]));
      lds_st16(sm, ex + (5 + rt * 2) * 1024, __builtin_bit_cast(u32x4, acc[rt][2]));
    }
  }
  {
    float sum = 0.f, sq = 0.f;
#pragma unroll
    for (int i = 0; i < 8; ++i) { u32x2 pp = lds_ld8(sm, Q_SP + (i * 64 + lane) * 8); sum += f_(pp[0]); sq += f_(pp[1]); }
    float mean, rinv; ln_stats(sum, sq, mean, rinv);
    lds_st4(sm, Q_MR + w * 512 + lane * 4, u_(mean)); lds_st4(sm, Q_MR + w * 512 + 256 + lane * 4, u_(rinv));
  }
  bar();
  STAMP(sb + 2);
  const int h = cb * 4 + hh;
  const u32 mrb = Q_MR + w * 512, csb = P_CS + hh * 2 * 3 * 16 * 4;
  if (kh == 1) {
    const f32x4 cs = __builtin_bit_cast(f32x4, lds_ld16(sm, csb + 16 * lq)) + __builtin_bit_cast(f32x4, lds_ld16(sm, csb + 192 + 16 * lq));
#pragma unroll
    for (int rp = 0; rp < 2; ++rp) {
      u32x4 ov;
#pragma unroll
      for (int t = 0; t < 2; ++t) {
        const int rt = rp * 2 + t;
        f32x4 a = acc[rt][0] + __builtin_bit_cast(f32x4, lds_ld16(sm, ex + rt * 1024));
        const float mean = f_(lds_ld4(sm, mrb + (rt * 16 + r16) * 4)), rinv = f_(lds_ld4(sm, mrb + 256 + (rt * 16 + r16) * 4));
        ov[2 * t] = phi2((lo2(a) - lo2(cs) * mean) * rinv); ov[2 * t + 1] = phi2((hi2(a) - hi2(cs) * mean) * rinv);
      }
      st16<SC1>(rw, o_pq + (((rb * 2 + rp) * 32 + h) * 64 + lane) * 16, ov);
    }
  } else {
    const float csk = f_(lds_ld4(sm, csb + 64 + r16 * 4)) + f_(lds_ld4(sm, csb + 192 + 64 + r16 * 4));
    const float csv = f_(lds_ld4(sm, csb + 128 + r16 * 4)) + f_(lds_ld4(sm, csb + 192 + 128 + r16 * 4));
    f32x4 kv = {0.f, 0.f, 0.f, 0.f}, zz = kv;
#pragma unroll
    for (int rp = 0; rp < 2; ++rp) {
      u32x4 pk, vv;
#pragma unroll
      for (int t = 0; t < 2; ++t) {
        const int rt = rp * 2 + t;
        f32x4 ak = acc[rt][1] + __builtin_bit_cast(f32x4, lds_ld16(sm, ex + (4 + rt * 2) * 1024));
        f32x4 av = acc[rt][2] + __builtin_bit_cast(f32x4, lds_ld16(sm, ex + (5 + rt * 2) * 1024));
        const f32x4 mean = __builtin_bit_cast(f32x4, lds_ld16(sm, mrb + (rt * 16 + 4 * lq) * 4));
        const f32x4 rinv = __builtin_bit_cast(f32x4, lds_ld16(sm, mrb + 256 + (rt * 16 + 4 * lq) * 4));
        pk[2 * t] = phi2((lo2(ak) - lo2(mean) * csk) * lo2(rinv)); pk[2 * t + 1] = phi2((hi2(ak) - hi2(mean) * csk) * hi2(rinv));
        f32x2 v0 = (lo2(av) - lo2(mean) * csv) * lo2(rinv), v1 = (hi2(av) - hi2(mean) * csv) * hi2(rinv);
        vv[2 * t] = pk2(v0[0], v0[1]); vv[2 * t + 1] = pk2(v1[0], v1[1]);
      }
      kv = mfma32(pk, vv, kv);
      zz = mfma32(pk, u32x4{0x3f803f80u, 0x3f803f80u, 0x3f803f80u, 0x3f803f80u}, zz);
    }
    st16<SC1>(rw, o_kvp + ((rb * 32 + h) * 64 + lane) * 16, __builtin_bit_cast(u32x4, kv));
    if (r16 == 0) st16<SC1>(rw, o_zp + (rb * 32 + h) * 64 + lq * 16, __builtin_bit_cast(u32x4, zz));
  }
  bar();
}

DEV void phase_R(rsrc_t rw, char* sm, int b, u32 o_kvp, u32 o_zp, u32 o_kvt, u32 o_zt) {
  const int tid = otid(), h = b >> 3, j = b & 7, rb = tid >> 4, q = tid & 15;
  u32x4 v[1];
  const u32 off = q < 8 ? o_kvp + ((rb * 32 + h) * 64 + 8 * j + q) * 16 : o_zp + (rb * 32 + h) * 64 + (j >> 1) * 16;
  if (RG) {
    if ((tid >> 6) == RG - 1) poll16<1, -1, true>(rw, [&](int) -> u32 { return off; }, v);
    asm volatile("s_barrier" ::: "memory");
  }
  poll16<1, -1, true>(rw, [&](int) -> u32 { return off; }, v);
  asm volatile("s_waitcnt vmcnt(0)" ::: "memory");
  if (q < 8) lds_st16(sm, R_B + (rb * 40 + q * 4) * 4, v[0]);
  else if (q == 8) lds_st8(sm, R_B + (rb * 40 + 32) * 4, (j & 1) ? u32x2{v[0][2], v[0][3]} : u32x2{v[0][0], v[0][1]});
  bar();
  if (tid < 64) {
    const int e = tid < 34 ? tid : 0;
    float s = 0.f;
#pragma unroll
    for (int r = 0; r < 32; ++r) s += f_(lds_ld4(sm, R_B + (r * 40 + e) * 4));
    float o = __shfl_down(s, 1), o2 = __shfl_down(s, 2), o3 = __shfl_down(s, 3);
    if (tid < 32 && (tid & 3) == 0) st8<SC1>(rw, o_kvt + (h * 64 + 8 * j + (tid >> 2)) * 8, u32x2{pk2(s, o), pk2(o2, o3)});
    if (tid == 32) st4<SC1>(rw, o_zt + h * 32 + j * 4, pk2(s, o));
  }
  bar();
}

DEV void o_prefetch(rsrc_t rw, u32x4 (&v)[5], int rb, int cb4, u32 o_pq, u32 o_xin) {
  const int tid = otid(), lane = tid & 63, w = tid >> 6;
#pragma unroll
  for (int i = 0; i < 4; ++i) v[i] = ld16<SC1>(rw, o_pq + ((rb * 32 + 4 * w + i) * 64 + lane) * 16);
  v[4] = ld16<SC1>(rw, o_xin + ((rb * 512 + cb4 * 128 + (tid >> 2)) * 32 + (tid & 3) * 8) * 2);
}
template <bool FINAL>
DEV void phase_O(unsigned short* out, rsrc_t rw, char* sm, const u32x4 (&Wo)[16], u32x4 (&v)[5], int rb, int cb4, u32 o_pq, u32 o_kvt, u32 o_zt,
                 u32 o_xin, u32 o_xout, u32 o_st, int sb STAMP_ARG) {
  const int tid = otid(), lane = tid & 63, w = tid >> 6, lq = lane >> 4, r16 = lane & 15;
  const int ecol = tid >> 2, epc = tid & 3;
  const u32 xoff = ((rb * 512 + cb4 * 128 + ecol) * 32 + epc * 8) * 2;
  u32x4 xin;
  {
    u32x2 kv[4], zt[4];
    if (!OG || w == OG - 1) {
      kv[3] = ld8<SC1>(rw, o_kvt + ((4 * w + 3) * 64 + lane) * 8);
      for (int spin = 0; spin < SPIN_CAP; ++spin) {
        if (!anyb(isbad(kv[3]))) break;
        __builtin_amdgcn_s_sleep(SLP);
        kv[3] = ld8<SC1>(rw, o_kvt + ((4 * w + 3) * 64 + lane) * 8);
      }
    }
    if (OG) asm volatile("s_barrier" ::: "memory");
#pragma unroll
    for (int i = 0; i < 4; ++i) { if (OG || i < 3) kv[i] = ld8<SC1>(rw, o_kvt + ((4 * w + i) * 64 + lane) * 8); zt[i] = ld8<SC1>(rw, o_zt + (4 * w + i) * 32 + lq * 8); }
    repoll16<5>(rw, [&](int i) -> u32 { return i < 4 ? o_pq + ((rb * 32 + 4 * w + i) * 64 + lane) * 16 : o_xin + xoff; }, v);
    xin = v[4];
    for (int spin = 0; spin < SPIN_CAP; ++spin) {
      bool bad = false;
#pragma unroll
      for (int i = 0; i < 4; ++i) bad = bad || isbad(kv[i]) || isbad(zt[i]);
      if (!anyb(bad)) break;
      __builtin_amdgcn_s_sleep(SLP);
#pragma unroll
      for (int i = 0; i < 4; ++i) { kv[i] = ld8<SC1>(rw, o_kvt + ((4 * w + i) * 64 + lane) * 8); zt[i] = ld8<SC1>(rw, o_zt + (4 * w + i) * 32 + lq * 8); }
    }
    STAMP(sb + 1);
#pragma unroll
    for (int hp = 0; hp < 2; ++hp)
#pragma unroll
      for (int rt = 0; rt < 2; ++rt) {
        u32x4 of;
#pragma unroll
        for (int t = 0; t < 2; ++t) {
          const int i = hp * 2 + t;
          u32x2 B = {v[i][2 * rt], v[i][2 * rt + 1]};
          f32x4 z4 = {0.f, 0.f, 0.f, 0.f};
          f32x4 num = mfma16(kv[i], B, z4), den = mfma16(zt[i], B, z4);
          const float dr = __builtin_amdgcn_rcpf(rbf(rbf(den[0]) + 1e-6f));
          f32x2 o0 = rbf2(lo2(num)) * dr, o1 = rbf2(hi2(num)) * dr;
          of[2 * t] = pk2(o0[0], o0[1]); of[2 * t + 1] = pk2(o1[0], o1[1]);
        }
        lds_st16(sm, O_OF + ((rt * 16 + 2 * w + hp) * 64 + lane) * 16, of);
      }
  }
  bar();
  {
    f32x4 a0 = {0.f, 0.f, 0.f, 0.f}, a1 = a0;
    u32x4 o0[16], o1[16];
#pragma unroll
    for (int ks = 0; ks < 4; ++ks) { o0[ks] = lds_ld16(sm, O_OF + (ks * 64 + lane) * 16); o1[ks] = lds_ld16(sm, O_OF + ((16 + ks) * 64 + lane) * 16); }
#pragma unroll
    for (int ks = 0; ks < 16; ++ks) {
      if (!FINAL && ks == 2) fill16(rw, O_PQ + XSZ + (u32)(blockIdx.x * NT + tid) * 16);
      if (ks + 4 < 16) { o0[ks + 4] = lds_ld16(sm, O_OF + ((ks + 4) * 64 + lane) * 16); o1[ks + 4] = lds_ld16(sm, O_OF + ((20 + ks) * 64 + lane) * 16); }
      sfence();
      a0 = mfma32(o0[ks], Wo[ks], a0);
      a1 = mfma32(o1[ks], Wo[ks], a1);
      sfence();
    }
    lds_st16(sm, O_T + (16 * w + r16) * 144 + (4 * lq) * 4, __builtin_bit_cast(u32x4, a0));
    lds_st16(sm, O_T + (16 * w + r16) * 144 + (16 + 4 * lq) * 4, __builtin_bit_cast(u32x4, a1));
  }
  bar();
  STAMP(sb + 2);
  f32x4 t0 = __builtin_bit_cast(f32x4, lds_ld16(sm, O_T + ecol * 144 + epc * 32)), t1 = __builtin_bit_cast(f32x4, lds_ld16(sm, O_T + ecol * 144 + epc * 32 + 16));
  u32x4 ov;
#pragma unroll
  for (int j = 0; j < 4; ++j) {
    f32x2 r = unpk(xin[j]) + rbf2(j == 0 ? lo2(t0) : j == 1 ? hi2(t0) : j == 2 ? lo2(t1) : hi2(t1));
    ov[j] = pk2(r[0], r[1]);
  }
  if (!FINAL) st16<SC1>(rw, o_xout + xoff, ov);
  lds_st16(sm, O_OF + ecol * 80 + epc * 16, ov);
  bar();
  const int rt = w & 1, cq = w >> 1;
  const u32 tb = O_OF + (cq * 32 + 8 * lq + (r16 >> 2)) * 80 + rt * 32 + (r16 & 3) * 8;
  const u32x2 c0 = lds_tr(sm, tb), c1 = lds_tr(sm, tb + 4 * 80);
  if (!FINAL) {
    float s = dot2(c0[0], 0x3f803f80u, 0.f), q = dot2(c0[0], c0[0], 0.f);
    s = dot2(c0[1], 0x3f803f80u, s); q = dot2(c0[1], c0[1], q);
    s = dot2(c1[0], 0x3f803f80u, s); q = dot2(c1[0], c1[0], q);
    s = dot2(c1[1], 0x3f803f80u, s); q = dot2(c1[1], c1[1], q);
    lds_st8(sm, O_PS + ((cq * 4 + lq) * 32 + rt * 16 + r16) * 8, u32x2{u_(s), u_(q)});
    bar();
    if (tid < 32) {
      float sum = 0.f, sq = 0.f;
#pragma unroll
      for (int i = 0; i < 16; ++i) { u32x2 pp = lds_ld8(sm, O_PS + (i * 32 + tid) * 8); sum += f_(pp[0]); sq += f_(pp[1]); }
      st8<SC1>(rw, o_st + (cb4 * L + rb * 32 + tid) * 8, u32x2{u_(sum), u_(sq)});
    }
    bar();
  } else {
    *(u32x4*)(out + (size_t)(rb * 32 + rt * 16 + r16) * 512 + cb4 * 128 + cq * 32 + lq * 8) = u32x4{c0[0], c0[1], c1[0], c1[1]};
  }
}

extern __shared__ __attribute__((aligned(16))) char smem[];
__global__ void __launch_bounds__(512, 2) kB(Params p) {
  STAMP_WS(p); STAMP(0);
  rsrc_t rw = mk_rsrc(p.ws, O_END);
  const int b = blockIdx.x, tid = otid();
  const int d0 = 2 * ((b & 7) * 32 + (b >> 3));
  const int rbq = (b & 7) * 4 + ((b >> 3) & 3), cb = b >> 5;
  const int rbo = (b & 7) * 8 + ((b >> 3) & 7), cb4 = b >> 6;
  const float dsk = bf2f(p.Dskip[d0 + (tid >> 8)]);
  u32x4 W[8][3], Wo[16], ov5[5], xv0, s0, s1;
  phase_A(p, rw, smem, d0, xv0, s0, s1 STAMP_PASS);
  STAMP(2);
  phase_C<1, false>(rw, smem, xv0, s0, s1, W, cb, d0, dsk, O_X0, O_ST0, O_X1, 3 STAMP_PASS);
  q_colsum(smem, W);
  STAMP(6);
  phase_Q<true>(rw, smem, W, Wo, rbq, cb, cb4, O_X1, O_PQ, O_KVP, O_ZP, 6 STAMP_PASS);
  STAMP(9);
  if (PF) o_prefetch(rw, ov5, rbo, cb4, O_PQ, O_X1);
  phase_R(rw, smem, b, O_KVP, O_ZP, O_KVT, O_ZT);
  if (!PF) o_prefetch(rw, ov5, rbo, cb4, O_PQ, O_X1);
  STAMP(10);
  phase_O<false>(p.out, rw, smem, Wo, ov5, rbo, cb4, O_PQ, O_KVT, O_ZT, O_X1, O_X2, O_ST2, 10 STAMP_PASS);
  STAMP(13);
  phase_C<4, true>(rw, smem, xv0, s0, s1, W, cb, d0, dsk, O_X2, O_ST2, O_X3, 13 STAMP_PASS);
  STAMP(16);
  phase_Q<false>(rw, smem, W, Wo, rbq, cb, cb4, O_X3, O_PQ + XSZ, O_KVP + KVP_SZ, O_ZP + ZP_SZ, 16 STAMP_PASS);
  STAMP(19);
  if (PF) o_prefetch(rw, ov5, rbo, cb4, O_PQ + XSZ, O_X3);
  phase_R(rw, smem, b, O_KVP + KVP_SZ, O_ZP + ZP_SZ, O_KVT + KVT_SZ, O_ZT + ZT_SZ);
  if (!PF) o_prefetch(rw, ov5, rbo, cb4, O_PQ + XSZ, O_X3);
  STAMP(20);
  phase_O<true>(p.out, rw, smem, Wo, ov5, rbo, cb4, O_PQ + XSZ, O_KVT + KVT_SZ, O_ZT + ZT_SZ, O_X3, 0u, 0u, 20 STAMP_PASS);
  STAMP(23);
}
static Params make_params(void* const* d_in, void* d_out, void* d_ws) {
  Params p;
  p.u = (const u32*)d_in[0]; p.logA = (const unsigned short*)d_in[1]; p.Aim = (const unsigned short*)d_in[2];
  p.Cre = (const unsigned short*)d_in[3]; p.Cim = (const unsigned short*)d_in[4]; p.logdt = (const unsigned short*)d_in[5];
  p.Dskip = (const unsigned short*)d_in[6]; p.wqkv = (const unsigned short*)d_in[8]; p.wo = (const unsigned short*)d_in[9];
  p.ws = (char*)d_ws; p.out = (unsigned short*)d_out;
  return p;
}
extern "C" void kernel_launch(void* const* d_in, const int* in_sizes, int n_in, void* d_out, int out_size, void* d_ws, size_t ws_size,
                              hipStream_t stream) {
  Params p = make_params(d_in, d_out, d_ws);
  (void)hipFuncSetAttribute((const void*)kB, hipFuncAttributeMaxDynamicSharedMemorySize, P_LDS);
  kB<<<NB, NT, P_LDS, stream>>>(p);
}
```

```cpp
#include <hip/hip_runtime.h>
#include <hip/hip_bf16.h>
#include <stdint.h>

typedef unsigned u32;
typedef unsigned u32x4 __attribute__((ext_vector_type(4)));
typedef unsigned u32x2 __attribute__((ext_vector_type(2)));
typedef float f32x4 __attribute__((ext_vector_type(4)));
typedef float f32x2 __attribute__((ext_vector_type(2)));
typedef short s16x4 __attribute__((ext_vector_type(4)));
typedef __bf16 bf16x8 __attribute__((ext_vector_type(8)));
typedef __bf16 bf16x2 __attribute__((ext_vector_type(2)));
#define LAS __attribute__((address_space(3)))
#define DEV __device__ __forceinline__

constexpr int L = 2048, D = 512, NB = 256, NT = 512;
constexpr u32 O_WQ = 0;
constexpr u32 O_WO = O_WQ + 1536 * 512 * 2;
constexpr u32 O_KR = O_WO + 512 * 512 * 2;
constexpr u32 KR_STR = 2064 * 2;
constexpr u32 XSZ = L * D * 2;
constexpr u32 O_X0 = O_KR + 512 * KR_STR;
constexpr u32 O_ST0 = O_X0 + XSZ;
constexpr u32 O_X1 = O_ST0 + L * 8;
constexpr u32 O_X2 = O_X1 + XSZ;
constexpr u32 O_X3 = O_X2 + XSZ;
constexpr u32 O_ST2 = O_X3 + XSZ;
constexpr u32 O_PQ = O_ST2 + 4 * L * 8;
constexpr u32 O_KVP = O_PQ + 2 * XSZ;
constexpr u32 KVP_SZ = 32 * 32 * 1024;
constexpr u32 O_ZP = O_KVP + 2 * KVP_SZ;
constexpr u32 ZP_SZ = 32 * 32 * 64;
constexpr u32 O_KVT = O_ZP + 2 * ZP_SZ;
constexpr u32 KVT_SZ = 32 * 512;
constexpr u32 O_ZT = O_KVT + 2 * KVT_SZ;
constexpr u32 ZT_SZ = 32 * 32;
constexpr u32 FLG_STR = 64;
constexpr u32 O_FLG = O_ZT + 2 * ZT_SZ;
constexpr u32 O_EPO = O_FLG + 256 * FLG_STR;
constexpr u32 O_END = O_EPO + 128;

struct Params {
  const u32* u; const unsigned short* logA; const unsigned short* Aim; const unsigned short* Cre; const unsigned short* Cim;
  const unsigned short* logdt; const unsigned short* Dskip; const unsigned short* wqkv; const unsigned short* wo;
  char* ws; unsigned short* out;
};

DEV float bf2f(u32 h) { return __builtin_bit_cast(float, h << 16); }
DEV float bflo(u32 w) { return __builtin_bit_cast(float, w << 16); }
DEV float bfhi(u32 w) { return __builtin_bit_cast(float, w & 0xffff0000u); }
DEV u32 pk2(float lo, float hi) { f32x2 f = {lo, hi}; return __builtin_bit_cast(u32, __builtin_convertvector(f, bf16x2)); }
DEV float rbf(float x) { return bflo(pk2(x, x)); }
DEV f32x4 mfma32(u32x4 a, u32x4 b, f32x4 c) {
  return __builtin_amdgcn_mfma_f32_16x16x32_bf16(__builtin_bit_cast(bf16x8, a), __builtin_bit_cast(bf16x8, b), c, 0, 0, 0);
}
DEV f32x4 mfma16(u32x2 a, u32x2 b, f32x4 c) {
  return __builtin_amdgcn_mfma_f32_16x16x16bf16_1k(__builtin_bit_cast(s16x4, a), __builtin_bit_cast(s16x4, b), c, 0, 0, 0);
}
DEV float dot2(u32 a, u32 b, float c) { return __builtin_amdgcn_fdot2_f32_bf16(__builtin_bit_cast(bf16x2, a), __builtin_bit_cast(bf16x2, b), c, false); }
typedef __amdgpu_buffer_rsrc_t rsrc_t;
DEV rsrc_t mk_rsrc(const void* p, u32 bytes) { return __builtin_amdgcn_make_buffer_rsrc((void*)p, (short)0, (int)bytes, 0x00020000); }
template <int AUX> DEV u32x4 ld16(rsrc_t r, u32 off) { return __builtin_amdgcn_raw_buffer_load_b128(r, off, 0, AUX); }
template <int AUX> DEV u32x2 ld8(rsrc_t r, u32 off) { return __builtin_amdgcn_raw_buffer_load_b64(r, off, 0, AUX); }
template <int AUX> DEV u32 ld4(rsrc_t r, u32 off) { return __builtin_amdgcn_raw_buffer_load_b32(r, off, 0, AUX); }
template <int AUX> DEV void st16(rsrc_t r, u32 off, u32x4 v) { __builtin_amdgcn_raw_buffer_store_b128(v, r, off, 0, AUX); }
template <int AUX> DEV void st8(rsrc_t r, u32 off, u32x2 v) { __builtin_amdgcn_raw_buffer_store_b64(v, r, off, 0, AUX); }
template <int AUX> DEV void st4(rsrc_t r, u32 off, u32 v) { __builtin_amdgcn_raw_buffer_store_b32(v, r, off, 0, AUX); }
DEV u32x4 lds_ld16(const char* s, u32 off) { return *(const LAS u32x4*)((const LAS char*)s + off); }
DEV u32x2 lds_ld8(const char* s, u32 off) { return *(const LAS u32x2*)((const LAS char*)s + off); }
DEV u32 lds_ld4(const char* s, u32 off) { return *(const LAS u32*)((const LAS char*)s + off); }
DEV void lds_st16(char* s, u32 off, u32x4 v) { *(LAS u32x4*)((LAS char*)s + off) = v; }
DEV void lds_st8(char* s, u32 off, u32x2 v) { *(LAS u32x2*)((LAS char*)s + off) = v; }
DEV void lds_st4(char* s, u32 off, u32 v) { *(LAS u32*)((LAS char*)s + off) = v; }
DEV u32x2 lds_tr(const char* s, u32 off) {
  return __builtin_bit_cast(u32x2, __builtin_amdgcn_ds_read_tr16_b64_v4i16((LAS s16x4*)((LAS char*)s + off)));
}
DEV float f_(u32 x) { return __builtin_bit_cast(float, x); }
DEV u32 u_(float x) { return __builtin_bit_cast(u32, x); }
template <int CTRL> DEV float dpp_add(float v) {
  return v + __builtin_bit_cast(float, __builtin_amdgcn_update_dpp(0, __builtin_bit_cast(int, v), CTRL, 0xf, 0xf, true));
}
DEV float row_sum16(float v) { v = dpp_add<0xB1>(v); v = dpp_add<0x4E>(v); v = dpp_add<0x141>(v); return dpp_add<0x140>(v); }
DEV float lane_of(float v, int l) { return __builtin_bit_cast(float, __builtin_amdgcn_readlane(__builtin_bit_cast(int, v), l)); }
DEV float xor16(float v) { return __shfl_xor(v, 16); }
DEV float xor32(float v) { return __shfl_xor(v, 32); }
DEV void bar() { asm volatile("s_waitcnt lgkmcnt(0)\n\ts_barrier" ::: "memory"); }
DEV int otid() { int t = threadIdx.x; asm volatile("" : "+v"(t)); return t; }
DEV void sfence() { __builtin_amdgcn_sched_barrier(0); }
constexpr u32 SENT = 0xffffffffu;
constexpr int SC1 = 16;
#define GC 8
#define GQ 0
#define GO 1
#define WC 0
#define WQ1 0
#define WO1 0
#define RG 0
#define OG 0
#define SLP 1
constexpr int SPIN_CAP = 1 << 14;
DEV bool isbad(u32x4 v) { return max(max(v[0], v[1]), max(v[2], v[3])) == SENT; }
DEV bool isbad(u32x2 v) { return max(v[0], v[1]) == SENT; }
DEV bool anyb(bool b) { return __builtin_amdgcn_ballot_w64(b) != 0ull; }
template <int N, class F> DEV void repoll16(rsrc_t r, F off, u32x4 (&v)[N]) {
  for (int spin = 0; spin < SPIN_CAP; ++spin) {
    u32 mask = 0;
#pragma unroll
    for (int i = 0; i < N; ++i) mask |= anyb(isbad(v[i])) ? (1u << i) : 0u;
    if (!mask) break;
    __builtin_amdgcn_s_sleep(SLP);
#pragma unroll
    for (int i = 0; i < N; ++i) if (mask & (1u << i)) v[i] = ld16<SC1>(r, off(i));
  }
}
template <int N, int G, bool OPT, class F> DEV void poll16(rsrc_t r, F off, u32x4 (&v)[N]) {
  constexpr int g = G >= 0 ? G : 0;
  if (OPT) {
#pragma unroll
    for (int i = 0; i < N; ++i) v[i] = ld16<SC1>(r, off(i));
  } else if (G >= 0) v[g] = ld16<SC1>(r, off(g));
  if (G >= 0) {
    for (int spin = 0; spin < SPIN_CAP; ++spin) {
      if (!anyb(isbad(v[g]))) break;
      __builtin_amdgcn_s_sleep(SLP);
      v[g] = ld16<SC1>(r, off(g));
    }
  }
  if (!OPT) {
#pragma unroll
    for (int i = 0; i < N; ++i) if (i != G) v[i] = ld16<SC1>(r, off(i));
  }
  repoll16<N>(r, off, v);
}
DEV int permk(int g, int j) { return j < 4 ? 4 * g + j : 16 + 4 * g + (j - 4); }
DEV void ln_stats(float sum, float sq, float& m, float& rinv) {
  float mean = sum * (1.0f / 512.0f);
  float var = fmaxf(sq * (1.0f / 512.0f) - mean * mean, 0.0f);
  m = rbf(mean);
  float den = rbf(__builtin_amdgcn_sqrtf(rbf(rbf(var) + 1e-5f)));
  rinv = __builtin_amdgcn_rcpf(den);
}
DEV f32x2 unpk(u32 w) { return f32x2{bflo(w), bfhi(w)}; }
DEV f32x2 rbf2(f32x2 v) { return unpk(pk2(v[0], v[1])); }
DEV f32x2 lo2(f32x4 v) { return f32x2{v[0], v[1]}; }
DEV f32x2 hi2(f32x4 v) { return f32x2{v[2], v[3]}; }
DEV u32 phi2(f32x2 a) {
  f32x2 t = rbf2(a), s = t * 1.4426950408889634f, p = t + 1.0f;
  f32x2 e = {__builtin_amdgcn_exp2f(s[0]), __builtin_amdgcn_exp2f(s[1])};
  f32x2 q = {t[0] > 0.0f ? p[0] : e[0], t[1] > 0.0f ? p[1] : e[1]};
  q = rbf2(q) + 1e-6f;
  return pk2(q[0], q[1]);
}
DEV float phi(float t) { float p = t > 0.0f ? t + 1.0f : __expf(t); return rbf(rbf(p) + 1e-6f); }
#define STAMP(i)
#define STAMP_WS(p)
#define STAMP_ARG
#define STAMP_PASS
constexpr u32 KA_ROW = 528;
constexpr u32 KA_TAB = 16 * KA_ROW;
constexpr u32 KA_G = 0, KA_E = 8 * KA_TAB;
constexpr u32 KA_KC = 16 * KA_TAB;
constexpr u32 KA_XT = KA_KC + 2 * 656 * 4;
constexpr u32 KA_LDS = KA_XT + 8 * 1040;
constexpr u32 KA_WIN = 0, KA_WSTR = 2064 * 16;
constexpr u32 KA_R = 100352;
#define KST 16
typedef f32x2 cf;
DEV cf cmul(cf a, cf b) { return f32x2{a[0], a[0]} * b + f32x2{a[1], a[1]} * f32x2{-b[1], b[0]}; }
DEV cf cexp_pow(float are, float aim, int k) {
  float s = (float)(1 << k);
  float mag = __expf(are * s);
  double rev = (double)aim * (0.15915494309189535 * (double)(1 << k));
  float fr = (float)(rev - __builtin_floor(rev));
  return f32x2{__builtin_amdgcn_cosf(fr), __builtin_amdgcn_sinf(fr)} * mag;
}
DEV void phase_A(const Params& p, rsrc_t rw, char* sm, int d0, u32x4& xv0, u32x4& s0, u32x4& s1 STAMP_ARG) {
  const int tid = otid(), b = blockIdx.x, lane = tid & 63, w = tid >> 6;
  const int n = tid & 127, ch = (tid >> 7) & 1, sih = tid >> 8;
  const int d = d0 + ch;
  const u32 rlA = p.logA[d * 128 + n], rAi = p.Aim[d * 128 + n], rCr = p.Cre[d * 128 + n], rCi = p.Cim[d * 128 + n], rdt = p.logdt[d];
  const u32 tag = ld4<SC1>(rw, O_EPO) + 1u;
  sfence();
  const int rg = (b & 7) * 32 + (b >> 3);
  const int row = rg * 8 + w;
  u32x4 xv = *(const u32x4*)(p.u + (size_t)row * 256 + lane * 4);
  u32 wv[8]; u32 wdst;
  {
    int g = b * NT + tid, ln = g & 63, lq = ln >> 4;
    const unsigned short* src; int ld, ks;
    if (g < 98304) {
      int f = g >> 6, ty = f % 3, f2 = f / 3, ks8 = f2 & 7, kh = (f2 >> 3) & 1, hh = (f2 >> 4) & 3, cb = f2 >> 6;
      ks = kh * 8 + ks8; src = p.wqkv + ty * 512 + (4 * cb + hh) * 16 + (ln & 15); ld = 1536; wdst = O_WQ + (u32)g * 16;
    } else {
      int g2 = g - 98304, f = g2 >> 6; ks = f & 15; int ww = (f >> 4) & 7, cb4 = f >> 7;
      src = p.wo + 128 * cb4 + 16 * ww + (ln & 15); ld = 512; wdst = O_WO + (u32)g2 * 16;
    }
#pragma unroll
    for (int j = 0; j < 8; ++j) wv[j] = src[(size_t)(32 * ks + permk(lq, j)) * ld];
  }
  {
    const u32 g = (u32)(b * NT + tid) * 16;
    const u32x4 sv = {SENT, SENT, SENT, SENT};
    st16<KST>(rw, O_X1 + g, sv);
    if (g < O_FLG - O_ZP) st16<KST>(rw, O_ZP + g, sv);
    if (g < 4 * L * 8) st16<KST>(rw, O_ST2 + g, sv);
  }
  sfence();
  const float lA = bf2f(rlA), Ai = bf2f(rAi), Cr = bf2f(rCr), Ci = bf2f(rCi), dt = rbf(expf(bf2f(rdt)));
  float are = -rbf(expf(lA)) * dt, aim = Ai * dt;
  cf C = {Cr * dt, Ci * dt};
#pragma unroll
  for (int t = 0; t < 2; ++t) {
    const int si = sih + 2 * t;
    const u32 tb = (u32)(ch * 4 + si) * KA_TAB + n * 4;
    cf v0 = cexp_pow(are, aim, si), v1 = cmul(v0, v0), v2 = cmul(v1, v1), v3 = cmul(v2, v2);
    cf e[8]; e[0] = f32x2{1.f, 0.f}; e[1] = v0; e[2] = v1; e[3] = cmul(v1, v0); e[4] = v2;
    e[5] = cmul(v2, v0); e[6] = cmul(v2, v1); e[7] = cmul(v2, e[3]);
#pragma unroll
    for (int j = 0; j < 8; ++j) {
      lds_st4(sm, KA_E + tb + j * KA_ROW, pk2(e[j][0], e[j][1]));
      cf f = cmul(e[j], v3);
      lds_st4(sm, KA_E + tb + (j + 8) * KA_ROW, pk2(f[0], f[1]));
    }
    cf g0 = cexp_pow(are, aim, 4 + si), g1 = cmul(g0, g0), g2 = cmul(g1, g1), g3 = cmul(g2, g2);
    cf h[8]; h[0] = (si == 0) ? C : cmul(C, g3);
    h[1] = cmul(h[0], g0); h[2] = cmul(h[0], g1); h[3] = cmul(h[1], g1);
#pragma unroll
    for (int j = 0; j < 4; ++j) h[4 + j] = cmul(h[j], g2);
#pragma unroll
    for (int j = 0; j < 8; ++j) {
      lds_st4(sm, KA_G + tb + j * KA_ROW, pk2(h[j][0], -h[j][1]));
      cf f = cmul(h[j], g3);
      lds_st4(sm, KA_G + tb + (j + 8) * KA_ROW, pk2(f[0], -f[1]));
    }
  }
  sfence();
  STAMP(24);
  {
    float s = 0.f, q = 0.f;
#pragma unroll
    for (int j = 0; j < 4; ++j) { s = dot2(xv[j], 0x3f803f80u, s); q = dot2(xv[j], xv[j], q); }
    s = row_sum16(s); q = row_sum16(q);
    s = (lane_of(s, 0) + lane_of(s, 16)) + (lane_of(s, 32) + lane_of(s, 48));
    q = (lane_of(q, 0) + lane_of(q, 16)) + (lane_of(q, 32) + lane_of(q, 48));
    if (lane == 0) { u32x2 v = {u_(s), u_(q)}; st8<KST>(rw, O_ST0 + row * 8, v); }
    lds_st16(sm, KA_XT + w * 1040 + lane * 16, xv);
  }
  { u32x4 v = {wv[0] | (wv[1] << 16), wv[2] | (wv[3] << 16), wv[4] | (wv[5] << 16), wv[6] | (wv[7] << 16)}; st16<KST>(rw, wdst, v); }
  bar();
  {
    u32 v[8];
#pragma unroll
    for (int r = 0; r < 8; ++r) v[r] = *(const LAS unsigned short*)((LAS char*)sm + KA_XT + r * 1040 + tid * 2);
    u32x4 o = {v[0] | (v[1] << 16), v[2] | (v[3] << 16), v[4] | (v[5] << 16), v[6] | (v[7] << 16)};
    st16<KST>(rw, O_X0 + (((rg >> 2) * 512 + tid) * 32 + (rg & 3) * 8) * 2, o);
  }
  sfence();
  STAMP(25);
  {
    const int wc = w >> 2, si = w & 3, lq = lane >> 4, r16 = lane & 15;
    const u32 base = (u32)(wc * 4 + si) * KA_TAB + r16 * KA_ROW + lq * 16;
    f32x4 acc = {0.f, 0.f, 0.f, 0.f};
#pragma unroll
    for (int ks = 0; ks < 8; ++ks) acc = mfma32(lds_ld16(sm, KA_G + base + ks * 64), lds_ld16(sm, KA_E + base + ks * 64), acc);
#pragma unroll
    for (int r = 0; r < 4; ++r) {
      int i = 4 * lq + r;
      int idx = si == 0 ? 16 * i + r16 : 256 + (si - 1) * 128 + 16 * i + r16;
      bool ok = si == 0 || i < 8 || (si == 3 && i == 8 && r16 == 0);
      if (ok) lds_st4(sm, KA_KC + (wc * 656 + idx) * 4, u_(2.0f * acc[r]));
    }
  }
  bar();
  const int c2 = tid >> 8, m = tid & 255;
  {
    u32 o[4];
#pragma unroll
    for (int j = 0; j < 4; ++j) {
      float kv[2];
#pragma unroll
      for (int h = 0; h < 2; ++h) {
        int l = 2047 - (8 * m + 2 * j + h);
        int e = l < 256 ? 0 : (l < 512 ? 1 : (l < 1024 ? 2 : 3));
        int mm = (l + ((1 << e) >> 1) - 1 + ((l >> e) & 1)) >> e;
        int idx = e == 0 ? l : 256 + (e - 1) * 128 + (mm - 128);
        kv[h] = f_(lds_ld4(sm, KA_KC + (c2 * 656 + idx) * 4));
      }
      o[j] = pk2(kv[0], kv[1]);
    }
    lds_st16(sm, KA_R + c2 * KR_STR + m * 16, u32x4{o[0], o[1], o[2], o[3]});
    if (m < 2) lds_st16(sm, KA_R + c2 * KR_STR + 4096 + m * 16, u32x4{0u, 0u, 0u, 0u});
  }
  asm volatile("s_waitcnt vmcnt(0)" ::: "memory");
  bar();
  if (tid == 0) st4<SC1>(rw, O_FLG + b * FLG_STR, tag);
  STAMP(26);
#pragma unroll
  for (int i = 0; i < 4; ++i) {
    const u32 rb_ = KA_R + c2 * KR_STR + (m + 256 * i) * 4;
    u32 a[5];
#pragma unroll
    for (int j = 0; j < 5; ++j) a[j] = lds_ld4(sm, rb_ + j * 4);
    const u32 gb = KA_WIN + c2 * KA_WSTR + (m + 256 * i) * 32;
    lds_st16(sm, gb, u32x4{a[0], a[1], a[2], a[3]});
    lds_st16(sm, gb + 16, u32x4{__builtin_amdgcn_alignbit(a[1], a[0], 16), __builtin_amdgcn_alignbit(a[2], a[1], 16),
                                __builtin_amdgcn_alignbit(a[3], a[2], 16), __builtin_amdgcn_alignbit(a[4], a[3], 16)});
  }
  if (m < 16) lds_st16(sm, KA_WIN + c2 * KA_WSTR + (2048 + m) * 16, u32x4{0u, 0u, 0u, 0u});
  if (w < 4) {
    u32 fl = ld4<SC1>(rw, O_FLG + tid * FLG_STR);
    for (int spin = 0; spin < SPIN_CAP; ++spin) {
      if (!anyb(fl != tag)) break;
      __builtin_amdgcn_s_sleep(SLP);
      fl = ld4<SC1>(rw, O_FLG + tid * FLG_STR);
    }
  }
  bar();
  if (b == 0 && tid == 0) st4<SC1>(rw, O_EPO, tag);
  STAMP(27);
  xv0 = ld16<SC1>(rw, O_X0 + (((m >> 2) * 512 + d0 + c2) * 32 + (m & 3) * 8) * 2);
  s0 = ld16<SC1>(rw, O_ST0 + tid * 32); s1 = ld16<SC1>(rw, O_ST0 + tid * 32 + 16);
}
#define PF 0
constexpr u32 P_G = 0, P_GSTR = 2064 * 16;
constexpr u32 DB = 2 * P_GSTR;
constexpr u32 P_CS = 158720;
constexpr u32 P_LDS = P_CS + 8 * 3 * 16 * 4;
constexpr u32 C_U = DB, C_ST = C_U + 2 * 4608, C_RED = C_ST + L * 8;
constexpr u32 Q_A = DB, Q_EX = DB, Q_SP = DB + 65536, Q_MR = Q_SP + 4096;
constexpr u32 O_OF = DB, O_T = DB + 32768, O_PS = O_T + 128 * 144;
constexpr u32 R_B = DB;
static_assert(KA_LDS <= P_CS && KA_WIN == P_G && KA_WSTR == P_GSTR && KA_R + 2 * KR_STR <= KA_KC && KA_R >= C_RED && C_RED + 65536 <= P_CS && Q_MR + 8 * 512 <= P_CS && P_LDS <= 163840, "LDS map");

DEV void fill16(rsrc_t rw, u32 off) { st16<SC1>(rw, off, u32x4{SENT, SENT, SENT, SENT}); }
template <int NP, bool POLL>
DEV void phase_C(rsrc_t rw, char* sm, u32x4 xv0, u32x4 s0, u32x4 s1, u32x4 (&W)[8][3], int cb, int d0, float dsk, u32 o_in, u32 o_st, u32 o_out, int sb STAMP_ARG) {
  const int tid = otid(), lane = tid & 63, w = tid >> 6, ch = tid >> 8, m = tid & 255;
  const u32 xoff = (((m >> 2) * 512 + d0 + ch) * 32 + (m & 3) * 8) * 2;
  u32x4 xv;
  {
    u32x4 sv[2 * NP + 1];
    auto off = [&](int i) -> u32 { return i == 2 * NP ? o_in + xoff : o_st + (i >> 1) * L * 8 + tid * 32 + (i & 1) * 16; };
    if (POLL) poll16<2 * NP + 1, (POLL ? GC : -1), WC>(rw, off, sv);
    else {
      sv[0] = s0; sv[1] = s1; sv[2 * NP] = xv0;
      const u32 fg = (u32)(blockIdx.x * NT + tid) * 16;
      fill16(rw, O_PQ + fg); fill16(rw, O_KVP + fg);
    }
    xv = sv[2 * NP];
    float mr[8];
#pragma unroll
    for (int r = 0; r < 4; ++r) {
      float s = 0.f, q2 = 0.f;
#pragma unroll
      for (int q = 0; q < NP; ++q) { s += f_(sv[2 * q + (r >> 1)][(r & 1) * 2]); q2 += f_(sv[2 * q + (r >> 1)][(r & 1) * 2 + 1]); }
      ln_stats(s, q2, mr[2 * r], mr[2 * r + 1]);
    }
    lds_st16(sm, C_ST + tid * 32, u32x4{u_(mr[0]), u_(mr[2]), u_(mr[1]), u_(mr[3])});
    lds_st16(sm, C_ST + tid * 32 + 16, u32x4{u_(mr[4]), u_(mr[6]), u_(mr[5]), u_(mr[7])});
    if (tid < 64) lds_st16(sm, C_U + (tid >> 5) * 4608 + ((tid >> 4) & 1) * 2304 + (tid & 15) * 16, u32x4{0u, 0u, 0u, 0u});
  }
  asm volatile("s_waitcnt vmcnt(0)" ::: "memory");
  bar();
  STAMP(sb + 1);
  u32x4 uv;
#pragma unroll
  for (int j = 0; j < 4; ++j) {
    u32x4 s = lds_ld16(sm, C_ST + (8 * m + 2 * j) * 8);
    f32x2 t = rbf2(unpk(xv[j]) - f32x2{f_(s[0]), f_(s[1])}) * f32x2{f_(s[2]), f_(s[3])};
    uv[j] = pk2(t[0], t[1]);
  }
  lds_st16(sm, C_U + ch * 4608 + (m & 1) * 2304 + (16 + (m >> 1)) * 16, uv);
  bar();
  {
    const int wc = w >> 2, w4 = w & 3, lq = lane >> 4, r16 = lane & 15;
    const int j0 = (lq & 1) * 16 + (lq >> 1) * 8, rho = 2 * w4;
    const u32 ub = C_U + wc * 4608 + (lq >> 1) * 2304 + (15 - 2 * rho + r16 + (lq & 1)) * 16;
    const u32 gb = P_G + wc * P_GSTR + (2031 - 32 * (rho + 1) - 256 * 7 - r16 + j0) * 16;
    f32x4 acc[8];
#pragma unroll
    for (int c = 0; c < 8; ++c) acc[c] = f32x4{0.f, 0.f, 0.f, 0.f};
    u32x4 B0[8], A0[8], B1[8], A1[8];
#pragma unroll
    for (int k = 0; k < 8; ++k) { if (k == 0) A0[0] = lds_ld16(sm, gb + 512 + 7 * 4096); B0[7 - k] = lds_ld16(sm, ub + (7 - k) * 256); }
#pragma unroll
    for (int k = 1; k < 8; ++k) A0[k] = lds_ld16(sm, gb + 512 + (7 - k) * 4096);
    sfence();
    const u32 wb = O_WQ + (u32)(cb * 8 + w) * 24576 + lane * 16;
#pragma unroll
    for (int g = 0; g < 8; ++g) {
      A1[g] = lds_ld16(sm, gb + (7 - g) * 4096); B1[g] = lds_ld16(sm, ub - 32 + g * 256);
#pragma unroll
      for (int i = 2 * g; i < 2 * g + 2; ++i) W[i / 3][i % 3] = ld16<SC1>(rw, wb + i * 1024);
      sfence();
#pragma unroll
      for (int c = 7; c >= g; --c) acc[c] = mfma32(A0[g], B0[c - g], acc[c]);
      sfence();
    }
#pragma unroll
    for (int g = 0; g < 8; ++g) {
      W[(16 + g) / 3][(16 + g) % 3] = ld16<SC1>(rw, wb + (16 + g) * 1024);
      sfence();
#pragma unroll
      for (int c = g; c < 8; ++c) acc[c] = mfma32(A1[g], B1[c - g], acc[c]);
      sfence();
    }
#pragma unroll
    for (int c = 0; c < 8; ++c) lds_st16(sm, C_RED + ((wc * 4 + w4) * 8 + c) * 1024 + lane * 16, __builtin_bit_cast(u32x4, acc[c]));
  }
  bar();
  STAMP(sb + 2);
  {
    const int c = m >> 5, a = (m & 31) >> 1, lq0 = (m & 1) * 2;
    f32x4 y0 = {0.f, 0.f, 0.f, 0.f}, y1 = y0;
#pragma unroll
    for (int q = 0; q < 4; ++q) {
      const u32 o = C_RED + ((ch * 4 + q) * 8 + c) * 1024 + (lq0 * 16 + a) * 16;
      y0 += __builtin_bit_cast(f32x4, lds_ld16(sm, o)); y1 += __builtin_bit_cast(f32x4, lds_ld16(sm, o + 256));
    }
    u32x4 ov;
#pragma unroll
    for (int j = 0; j < 4; ++j) {
      f32x2 y = j == 0 ? lo2(y0) : j == 1 ? hi2(y0) : j == 2 ? lo2(y1) : hi2(y1);
      f32x2 r = unpk(xv[j]) + rbf2(rbf2(y) + rbf2(unpk(uv[j]) * dsk));
      ov[j] = pk2(r[0], r[1]);
    }
    st16<SC1>(rw, o_out + xoff, ov);
  }
  bar();
}

DEV void q_colsum(char* sm, const u32x4 (&W)[8][3]) {
  const int tid = otid(), lane = tid & 63, w = tid >> 6;
#pragma unroll
  for (int ty = 0; ty < 3; ++ty) {
    float s = 0.f;
#pragma unroll
    for (int ks = 0; ks < 8; ++ks)
#pragma unroll
      for (int j = 0; j < 4; ++j) s = dot2(W[ks][ty][j], 0x3f803f80u, s);
    s += xor16(s); s += xor32(s);
    if (lane < 16) lds_st4(sm, P_CS + ((w * 3 + ty) * 16 + lane) * 4, u_(s));
  }
}
template <bool FIRST>
DEV void phase_Q(rsrc_t rw, char* sm, const u32x4 (&W)[8][3], u32x4 (&Wo)[16], int rb, int cb, int cb4, u32 o_x, u32 o_pq, u32 o_kvp, u32 o_zp,
                 int sb STAMP_ARG) {
  const int tid = otid(), lane = tid & 63, w = tid >> 6, hh = w >> 1, kh = w & 1, lq = lane >> 4, r16 = lane & 15;
  {
    u32x4 av[8];
    poll16<8, GQ, WQ1>(rw, [&](int i) -> u32 { return o_x + (u32)rb * 65536 + (tid + i * 512) * 16; }, av);
    const int chn = tid >> 2, pc = tid & 3, f = (chn >> 1) & 3;
#pragma unroll
    for (int i = 0; i < 8; ++i)
      lds_st16(sm, Q_A + (chn + 128 * (i & 3)) * 128 + ((((i >> 2) * 2 + (pc >> 1)) ^ f) * 32) + (pc & 1) * 16, av[i]);
  }
  bar();
  STAMP(sb + 1);
  f32x4 acc[4][3];
#pragma unroll
  for (int rt = 0; rt < 4; ++rt)
#pragma unroll
    for (int ty = 0; ty < 3; ++ty) acc[rt][ty] = f32x4{0.f, 0.f, 0.f, 0.f};
  const int chl = 4 * lq + (r16 >> 2), f = (chl >> 1) & 3;
  u32 ab[4];
#pragma unroll
  for (int rt = 0; rt < 4; ++rt) ab[rt] = Q_A + (kh * 256 + chl) * 128 + ((rt ^ f) * 32) + (r16 & 3) * 8;
  const u32 abs_ = Q_A + (kh * 256 + chl) * 128 + ((hh ^ f) * 32) + (r16 & 3) * 8;
  float ssum = 0.f, ssq = 0.f;
  const u32 wob = O_WO + (u32)(cb4 * 8 + w) * 16384 + lane * 16;
  {
    u32x2 xa[2][4][2], xs[2][2];
#pragma unroll
    for (int rt = 0; rt < 4; ++rt) { xa[0][rt][0] = lds_tr(sm, ab[rt]); xa[0][rt][1] = lds_tr(sm, ab[rt] + 2048); }
    xs[0][0] = lds_tr(sm, abs_); xs[0][1] = lds_tr(sm, abs_ + 2048);
#pragma unroll
    for (int ks = 0; ks < 8; ++ks) {
      const int cu = ks & 1, nx = cu ^ 1;
      if (ks < 7) {
#pragma unroll
        for (int rt = 0; rt < 4; ++rt) { xa[nx][rt][0] = lds_tr(sm, ab[rt] + (ks + 1) * 4096); xa[nx][rt][1] = lds_tr(sm, ab[rt] + (ks + 1) * 4096 + 2048); }
        xs[nx][0] = lds_tr(sm, abs_ + (ks + 1) * 4096); xs[nx][1] = lds_tr(sm, abs_ + (ks + 1) * 4096 + 2048);
      }
      Wo[2 * ks] = ld16<SC1>(rw, wob + 2 * ks * 1024); Wo[2 * ks + 1] = ld16<SC1>(rw, wob + (2 * ks + 1) * 1024);
      if (FIRST && ks == 1) fill16(rw, O_X2 + (u32)(blockIdx.x * NT + tid) * 16);
      if (FIRST && ks == 4) fill16(rw, O_X3 + (u32)(blockIdx.x * NT + tid) * 16);
      sfence();
#pragma unroll
      for (int rt = 0; rt < 4; ++rt) {
        u32x4 X = {xa[cu][rt][0][0], xa[cu][rt][0][1], xa[cu][rt][1][0], xa[cu][rt][1][1]};
        acc[rt][0] = mfma32(W[ks][0], X, acc[rt][0]);
        acc[rt][1] = mfma32(X, W[ks][1], acc[rt][1]);
        acc[rt][2] = mfma32(X, W[ks][2], acc[rt][2]);
      }
#pragma unroll
      for (int j = 0; j < 2; ++j)
#pragma unroll
        for (int e = 0; e < 2; ++e) { ssum = dot2(xs[cu][j][e], 0x3f803f80u, ssum); ssq = dot2(xs[cu][j][e], xs[cu][j][e], ssq); }
      sfence();
    }
  }
  lds_st8(sm, Q_SP + ((kh * 4 + lq) * 64 + hh * 16 + r16) * 8, u32x2{u_(ssum), u_(ssq)});
  bar();
  const u32 ex = Q_EX + hh * 12288 + lane * 16;
  if (kh == 0) {
#pragma unroll
    for (int rt = 0; rt < 4; ++rt) lds_st16(sm, ex + rt * 1024, __builtin_bit_cast(u32x4, acc[rt][0]));
  } else {
#pragma unroll
    for (int rt = 0; rt < 4; ++rt) {
      lds_st16(sm, ex + (4 + rt * 2) * 1024, __builtin_bit_cast(u32x4, acc[rt][1]));
      lds_st16(sm, ex + (5 + rt * 2) * 1024, __builtin_bit_cast(u32x4, acc[rt][2]));
    }
  }
  {
    float sum = 0.f, sq = 0.f;
#pragma unroll
    for (int i = 0; i < 8; ++i) { u32x2 pp = lds_ld8(sm, Q_SP + (i * 64 + lane) * 8); sum += f_(pp[0]); sq += f_(pp[1]); }
    float mean, rinv; ln_stats(sum, sq, mean, rinv);
    lds_st4(sm, Q_MR + w * 512 + lane * 4, u_(mean)); lds_st4(sm, Q_MR + w * 512 + 256 + lane * 4, u_(rinv));
  }
  bar();
  STAMP(sb + 2);
  const int h = cb * 4 + hh;
  const u32 mrb = Q_MR + w * 512, csb = P_CS + hh * 2 * 3 * 16 * 4;
  if (kh == 1) {
    const f32x4 cs = __builtin_bit_cast(f32x4, lds_ld16(sm, csb + 16 * lq)) + __builtin_bit_cast(f32x4, lds_ld16(sm, csb + 192 + 16 * lq));
#pragma unroll
    for (int rp = 0; rp < 2; ++rp) {
      u32x4 ov;
#pragma unroll
      for (int t = 0; t < 2; ++t) {
        const int rt = rp * 2 + t;
        f32x4 a = acc[rt][0] + __builtin_bit_cast(f32x4, lds_ld16(sm, ex + rt * 1024));
        const float mean = f_(lds_ld4(sm, mrb + (rt * 16 + r16) * 4)), rinv = f_(lds_ld4(sm, mrb + 256 + (rt * 16 + r16) * 4));
        ov[2 * t] = phi2((lo2(a) - lo2(cs) * mean) * rinv); ov[2 * t + 1] = phi2((hi2(a) - hi2(cs) * mean) * rinv);
      }
      st16<SC1>(rw, o_pq + (((rb * 2 + rp) * 32 + h) * 64 + lane) * 16, ov);
    }
  } else {
    const float csk = f_(lds_ld4(sm, csb + 64 + r16 * 4)) + f_(lds_ld4(sm, csb + 192 + 64 + r16 * 4));
    const float csv = f_(lds_ld4(sm, csb + 128 + r16 * 4)) + f_(lds_ld4(sm, csb + 192 + 128 + r16 * 4));
    f32x4 kv = {0.f, 0.f, 0.f, 0.f}, zz = kv;
#pragma unroll
    for (int rp = 0; rp < 2; ++rp) {
      u32x4 pk, vv;
#pragma unroll
      for (int t = 0; t < 2; ++t) {
        const int rt = rp * 2 + t;
        f32x4 ak = acc[rt][1] + __builtin_bit_cast(f32x4, lds_ld16(sm, ex + (4 + rt * 2) * 1024));
        f32x4 av = acc[rt][2] + __builtin_bit_cast(f32x4, lds_ld16(sm, ex + (5 + rt * 2) * 1024));
        const f32x4 mean = __builtin_bit_cast(f32x4, lds_ld16(sm, mrb + (rt * 16 + 4 * lq) * 4));
        const f32x4 rinv = __builtin_bit_cast(f32x4, lds_ld16(sm, mrb + 256 + (rt * 16 + 4 * lq) * 4));
        pk[2 * t] = phi2((lo2(ak) - lo2(mean) * csk) * lo2(rinv)); pk[2 * t + 1] = phi2((hi2(ak) - hi2(mean) * csk) * hi2(rinv));
        f32x2 v0 = (lo2(av) - lo2(mean) * csv) * lo2(rinv), v1 = (hi2(av) - hi2(mean) * csv) * hi2(rinv);
        vv[2 * t] = pk2(v0[0], v0[1]); vv[2 * t + 1] = pk2(v1[0], v1[1]);
      }
      kv = mfma32(pk, vv, kv);
      zz = mfma32(pk, u32x4{0x3f803f80u, 0x3f803f80u, 0x3f803f80u, 0x3f803f80u}, zz);
    }
    st16<SC1>(rw, o_kvp + ((rb * 32 + h) * 64 + lane) * 16, __builtin_bit_cast(u32x4, kv));
    if (r16 == 0) st16<SC1>(rw, o_zp + (rb * 32 + h) * 64 + lq * 16, __builtin_bit_cast(u32x4, zz));
  }
  bar();
}

DEV void phase_R(rsrc_t rw, char* sm, int b, u32 o_kvp, u32 o_zp, u32 o_kvt, u32 o_zt) {
  const int tid = otid(), h = b >> 3, j = b & 7, rb = tid >> 4, q = tid & 15;
  u32x4 v[1];
  const u32 off = q < 8 ? o_kvp + ((rb * 32 + h) * 64 + 8 * j + q) * 16 : o_zp + (rb * 32 + h) * 64 + (j >> 1) * 16;
  if (RG) {
    if ((tid >> 6) == RG - 1) poll16<1, -1, true>(rw, [&](int) -> u32 { return off; }, v);
    asm volatile("s_barrier" ::: "memory");
  }
  poll16<1, -1, true>(rw, [&](int) -> u32 { return off; }, v);
  asm volatile("s_waitcnt vmcnt(0)" ::: "memory");
  if (q < 8) lds_st16(sm, R_B + (rb * 40 + q * 4) * 4, v[0]);
  else if (q == 8) lds_st8(sm, R_B + (rb * 40 + 32) * 4, (j & 1) ? u32x2{v[0][2], v[0][3]} : u32x2{v[0][0], v[0][1]});
  bar();
  if (tid < 64) {
    const int e = tid < 34 ? tid : 0;
    float s = 0.f;
#pragma unroll
    for (int r = 0; r < 32; ++r) s += f_(lds_ld4(sm, R_B + (r * 40 + e) * 4));
    float o = __shfl_down(s, 1), o2 = __shfl_down(s, 2), o3 = __shfl_down(s, 3);
    if (tid < 32 && (tid & 3) == 0) st8<SC1>(rw, o_kvt + (h * 64 + 8 * j + (tid >> 2)) * 8, u32x2{pk2(s, o), pk2(o2, o3)});
    if (tid == 32) st4<SC1>(rw, o_zt + h * 32 + j * 4, pk2(s, o));
  }
  bar();
}

DEV void o_prefetch(rsrc_t rw, u32x4 (&v)[5], int rb, int cb4, u32 o_pq, u32 o_xin) {
  const int tid = otid(), lane = tid & 63, w = tid >> 6;
#pragma unroll
  for (int i = 0; i < 4; ++i) v[i] = ld16<SC1>(rw, o_pq + ((rb * 32 + 4 * w + i) * 64 + lane) * 16);
  v[4] = ld16<SC1>(rw, o_xin + ((rb * 512 + cb4 * 128 + (tid >> 2)) * 32 + (tid & 3) * 8) * 2);
}
template <bool FINAL>
DEV void phase_O(unsigned short* out, rsrc_t rw, char* sm, const u32x4 (&Wo)[16], u32x4 (&v)[5], int rb, int cb4, u32 o_pq, u32 o_kvt, u32 o_zt,
                 u32 o_xin, u32 o_xout, u32 o_st, int sb STAMP_ARG) {
  const int tid = otid(), lane = tid & 63, w = tid >> 6, lq = lane >> 4, r16 = lane & 15;
  const int ecol = tid >> 2, epc = tid & 3;
  const u32 xoff = ((rb * 512 + cb4 * 128 + ecol) * 32 + epc * 8) * 2;
  u32x4 xin;
  {
    u32x2 kv[4], zt[4];
    if (!OG || w == OG - 1) {
      kv[3] = ld8<SC1>(rw, o_kvt + ((4 * w + 3) * 64 + lane) * 8);
      for (int spin = 0; spin < SPIN_CAP; ++spin) {
        if (!anyb(isbad(kv[3]))) break;
        __builtin_amdgcn_s_sleep(SLP);
        kv[3] = ld8<SC1>(rw, o_kvt + ((4 * w + 3) * 64 + lane) * 8);
      }
    }
    if (OG) asm volatile("s_barrier" ::: "memory");
#pragma unroll
    for (int i = 0; i < 4; ++i) { if (OG || i < 3) kv[i] = ld8<SC1>(rw, o_kvt + ((4 * w + i) * 64 + lane) * 8); zt[i] = ld8<SC1>(rw, o_zt + (4 * w + i) * 32 + lq * 8); }
    repoll16<5>(rw, [&](int i) -> u32 { return i < 4 ? o_pq + ((rb * 32 + 4 * w + i) * 64 + lane) * 16 : o_xin + xoff; }, v);
    xin = v[4];
    for (int spin = 0; spin < SPIN_CAP; ++spin) {
      bool bad = false;
#pragma unroll
      for (int i = 0; i < 4; ++i) bad = bad || isbad(kv[i]) || isbad(zt[i]);
      if (!anyb(bad)) break;
      __builtin_amdgcn_s_sleep(SLP);
#pragma unroll
      for (int i = 0; i < 4; ++i) { kv[i] = ld8<SC1>(rw, o_kvt + ((4 * w + i) * 64 + lane) * 8); zt[i] = ld8<SC1>(rw, o_zt + (4 * w + i) * 32 + lq * 8); }
    }
    STAMP(sb + 1);
#pragma unroll
    for (int hp = 0; hp < 2; ++hp)
#pragma unroll
      for (int rt = 0; rt < 2; ++rt) {
        u32x4 of;
#pragma unroll
        for (int t = 0; t < 2; ++t) {
          const int i = hp * 2 + t;
          u32x2 B = {v[i][2 * rt], v[i][2 * rt + 1]};
          f32x4 z4 = {0.f, 0.f, 0.f, 0.f};
          f32x4 num = mfma16(kv[i], B, z4), den = mfma16(zt[i], B, z4);
          const float dr = __builtin_amdgcn_rcpf(rbf(rbf(den[0]) + 1e-6f));
          f32x2 o0 = rbf2(lo2(num)) * dr, o1 = rbf2(hi2(num)) * dr;
          of[2 * t] = pk2(o0[0], o0[1]); of[2 * t + 1] = pk2(o1[0], o1[1]);
        }
        lds_st16(sm, O_OF + ((rt * 16 + 2 * w + hp) * 64 + lane) * 16, of);
      }
  }
  bar();
  {
    f32x4 a0 = {0.f, 0.f, 0.f, 0.f}, a1 = a0;
    u32x4 o0[16], o1[16];
#pragma unroll
    for (int ks = 0; ks < 4; ++ks) { o0[ks] = lds_ld16(sm, O_OF + (ks * 64 + lane) * 16); o1[ks] = lds_ld16(sm, O_OF + ((16 + ks) * 64 + lane) * 16); }
#pragma unroll
    for (int ks = 0; ks < 16; ++ks) {
      if (!FINAL && ks == 2) fill16(rw, O_PQ + XSZ + (u32)(blockIdx.x * NT + tid) * 16);
      if (ks + 4 < 16) { o0[ks + 4] = lds_ld16(sm, O_OF + ((ks + 4) * 64 + lane) * 16); o1[ks + 4] = lds_ld16(sm, O_OF + ((20 + ks) * 64 + lane) * 16); }
      sfence();
      a0 = mfma32(o0[ks], Wo[ks], a0);
      a1 = mfma32(o1[ks], Wo[ks], a1);
      sfence();
    }
    lds_st16(sm, O_T + (16 * w + r16) * 144 + (4 * lq) * 4, __builtin_bit_cast(u32x4, a0));
    lds_st16(sm, O_T + (16 * w + r16) * 144 + (16 + 4 * lq) * 4, __builtin_bit_cast(u32x4, a1));
  }
  bar();
  STAMP(sb + 2);
  f32x4 t0 = __builtin_bit_cast(f32x4, lds_ld16(sm, O_T + ecol * 144 + epc * 32)), t1 = __builtin_bit_cast(f32x4, lds_ld16(sm, O_T + ecol * 144 + epc * 32 + 16));
  u32x4 ov;
#pragma unroll
  for (int j = 0; j < 4; ++j) {
    f32x2 r = unpk(xin[j]) + rbf2(j == 0 ? lo2(t0) : j == 1 ? hi2(t0) : j == 2 ? lo2(t1) : hi2(t1));
    ov[j] = pk2(r[0], r[1]);
  }
  if (!FINAL) st16<SC1>(rw, o_xout + xoff, ov);
  lds_st16(sm, O_OF + ecol * 80 + epc * 16, ov);
  bar();
  const int rt = w & 1, cq = w >> 1;
  const u32 tb = O_OF + (cq * 32 + 8 * lq + (r16 >> 2)) * 80 + rt * 32 + (r16 & 3) * 8;
  const u32x2 c0 = lds_tr(sm, tb), c1 = lds_tr(sm, tb + 4 * 80);
  if (!FINAL) {
    float s = dot2(c0[0], 0x3f803f80u, 0.f), q = dot2(c0[0], c0[0], 0.f);
    s = dot2(c0[1], 0x3f803f80u, s); q = dot2(c0[1], c0[1], q);
    s = dot2(c1[0], 0x3f803f80u, s); q = dot2(c1[0], c1[0], q);
    s = dot2(c1[1], 0x3f803f80u, s); q = dot2(c1[1], c1[1], q);
    lds_st8(sm, O_PS + ((cq * 4 + lq) * 32 + rt * 16 + r16) * 8, u32x2{u_(s), u_(q)});
    bar();
    if (tid < 32) {
      float sum = 0.f, sq = 0.f;
#pragma unroll
      for (int i = 0; i < 16; ++i) { u32x2 pp = lds_ld8(sm, O_PS + (i * 32 + tid) * 8); sum += f_(pp[0]); sq += f_(pp[1]); }
      st8<SC1>(rw, o_st + (cb4 * L + rb * 32 + tid) * 8, u32x2{u_(sum), u_(sq)});
    }
    bar();
  } else {
    *(u32x4*)(out + (size_t)(rb * 32 + rt * 16 + r16) * 512 + cb4 * 128 + cq * 32 + lq * 8) = u32x4{c0[0], c0[1], c1[0], c1[1]};
  }
}

extern __shared__ __attribute__((aligned(16))) char smem[];
__global__ void __launch_bounds__(512, 2) kB(Params p) {
  STAMP_WS(p); STAMP(0);
  rsrc_t rw = mk_rsrc(p.ws, O_END);
  const int b = blockIdx.x, tid = otid();
  const int d0 = 2 * ((b & 7) * 32 + (b >> 3));
  const int rbq = (b & 7) * 4 + ((b >> 3) & 3), cb = b >> 5;
  const int rbo = (b & 7) * 8 + ((b >> 3) & 7), cb4 = b >> 6;
  const float dsk = bf2f(p.Dskip[d0 + (tid >> 8)]);
  u32 pcv;
  {
    unsigned long long pc; u32 len;
    asm volatile("s_getpc_b64 %0\nkb_text_begin:\n\ts_mov_b32 %1, kb_text_end-kb_text_begin" : "=s"(pc), "=s"(len));
    const u32 o = ((u32)(b >> 3) * NT + tid) * 4;
    pcv = *(const __attribute__((address_space(1))) u32*)(pc + min(o, len - 4u));
  }
  u32x4 W[8][3], Wo[16], ov5[5], xv0, s0, s1;
  phase_A(p, rw, smem, d0, xv0, s0, s1 STAMP_PASS);
  asm volatile("" :: "v"(pcv));
  STAMP(2);
  phase_C<1, false>(rw, smem, xv0, s0, s1, W, cb, d0, dsk, O_X0, O_ST0, O_X1, 3 STAMP_PASS);
  q_colsum(smem, W);
  STAMP(6);
  phase_Q<true>(rw, smem, W, Wo, rbq, cb, cb4, O_X1, O_PQ, O_KVP, O_ZP, 6 STAMP_PASS);
  STAMP(9);
  if (PF) o_prefetch(rw, ov5, rbo, cb4, O_PQ, O_X1);
  phase_R(rw, smem, b, O_KVP, O_ZP, O_KVT, O_ZT);
  if (!PF) o_prefetch(rw, ov5, rbo, cb4, O_PQ, O_X1);
  STAMP(10);
  phase_O<false>(p.out, rw, smem, Wo, ov5, rbo, cb4, O_PQ, O_KVT, O_ZT, O_X1, O_X2, O_ST2, 10 STAMP_PASS);
  STAMP(13);
  phase_C<4, true>(rw, smem, xv0, s0, s1, W, cb, d0, dsk, O_X2, O_ST2, O_X3, 13 STAMP_PASS);
  STAMP(16);
  phase_Q<false>(rw, smem, W, Wo, rbq, cb, cb4, O_X3, O_PQ + XSZ, O_KVP + KVP_SZ, O_ZP + ZP_SZ, 16 STAMP_PASS);
  STAMP(19);
  if (PF) o_prefetch(rw, ov5, rbo, cb4, O_PQ + XSZ, O_X3);
  phase_R(rw, smem, b, O_KVP + KVP_SZ, O_ZP + ZP_SZ, O_KVT + KVT_SZ, O_ZT + ZT_SZ);
  if (!PF) o_prefetch(rw, ov5, rbo, cb4, O_PQ + XSZ, O_X3);
  STAMP(20);
  phase_O<true>(p.out, rw, smem, Wo, ov5, rbo, cb4, O_PQ + XSZ, O_KVT + KVT_SZ, O_ZT + ZT_SZ, O_X3, 0u, 0u, 20 STAMP_PASS);
  STAMP(23);
  asm volatile("kb_text_end:");
}
static Params make_params(void* const* d_in, void* d_out, void* d_ws) {
  Params p;
  p.u = (const u32*)d_in[0]; p.logA = (const unsigned short*)d_in[1]; p.Aim = (const unsigned short*)d_in[2];
  p.Cre = (const unsigned short*)d_in[3]; p.Cim = (const unsigned short*)d_in[4]; p.logdt = (const unsigned short*)d_in[5];
  p.Dskip = (const unsigned short*)d_in[6]; p.wqkv = (const unsigned short*)d_in[8]; p.wo = (const unsigned short*)d_in[9];
  p.ws = (char*)d_ws; p.out = (unsigned short*)d_out;
  return p;
}
extern "C" void kernel_launch(void* const* d_in, const int* in_sizes, int n_in, void* d_out, int out_size, void* d_ws, size_t ws_size,
                              hipStream_t stream) {
  Params p = make_params(d_in, d_out, d_ws);
  (void)hipFuncSetAttribute((const void*)kB, hipFuncAttributeMaxDynamicSharedMemorySize, P_LDS);
  kB<<<NB, NT, P_LDS, stream>>>(p);
}
```

```cpp
#include <hip/hip_runtime.h>
#include <hip/hip_bf16.h>
#include <stdint.h>

typedef unsigned u32;
typedef unsigned u32x4 __attribute__((ext_vector_type(4)));
typedef unsigned u32x2 __attribute__((ext_vector_type(2)));
typedef float f32x4 __attribute__((ext_vector_type(4)));
typedef float f32x2 __attribute__((ext_vector_type(2)));
typedef short s16x4 __attribute__((ext_vector_type(4)));
typedef __bf16 bf16x8 __attribute__((ext_vector_type(8)));
typedef __bf16 bf16x2 __attribute__((ext_vector_type(2)));
#define LAS __attribute__((address_space(3)))
#define DEV __device__ __forceinline__

constexpr int L = 2048, D = 512, NB = 256, NT = 512;
constexpr u32 O_WQ = 0;
constexpr u32 O_WO = O_WQ + 1536 * 512 * 2;
constexpr u32 KR_STR = 2064 * 2;
constexpr u32 XSZ = L * D * 2;
constexpr u32 O_X0 = O_WO + 512 * 512 * 2;
constexpr u32 O_ST0 = O_X0 + XSZ;
constexpr u32 O_X1 = O_ST0 + L * 8;
constexpr u32 O_X2 = O_X1 + XSZ;
constexpr u32 O_X3 = O_X2 + XSZ;
constexpr u32 O_ST2 = O_X3 + XSZ;
constexpr u32 O_PQ = O_ST2 + 4 * L * 8;
constexpr u32 O_KVP = O_PQ + 2 * XSZ;
constexpr u32 KVP_SZ = 32 * 32 * 1024;
constexpr u32 O_ZP = O_KVP + 2 * KVP_SZ;
constexpr u32 ZP_SZ = 32 * 32 * 64;
constexpr u32 O_KVT = O_ZP + 2 * ZP_SZ;
constexpr u32 KVT_SZ = 32 * 512;
constexpr u32 O_ZT = O_KVT + 2 * KVT_SZ;
constexpr u32 ZT_SZ = 32 * 32;
constexpr u32 FLG_STR = 64;
constexpr u32 O_FLG = O_ZT + 2 * ZT_SZ;
constexpr u32 O_EPO = O_FLG + 256 * FLG_STR;
constexpr u32 O_END = O_EPO + 128;

struct Params {
  const u32* u; const unsigned short* logA; const unsigned short* Aim; const unsigned short* Cre; const unsigned short* Cim;
  const unsigned short* logdt; const unsigned short* Dskip; const unsigned short* wqkv; const unsigned short* wo;
  char* ws; unsigned short* out;
};

DEV float bf2f(u32 h) { return __builtin_bit_cast(float, h << 16); }
DEV float bflo(u32 w) { return __builtin_bit_cast(float, w << 16); }
DEV float bfhi(u32 w) { return __builtin_bit_cast(float, w & 0xffff0000u); }
DEV u32 pk2(float lo, float hi) { f32x2 f = {lo, hi}; return __builtin_bit_cast(u32, __builtin_convertvector(f, bf16x2)); }
DEV float rbf(float x) { return bflo(pk2(x, x)); }
DEV f32x4 mfma32(u32x4 a, u32x4 b, f32x4 c) {
  return __builtin_amdgcn_mfma_f32_16x16x32_bf16(__builtin_bit_cast(bf16x8, a), __builtin_bit_cast(bf16x8, b), c, 0, 0, 0);
}
DEV f32x4 mfma16(u32x2 a, u32x2 b, f32x4 c) {
  return __builtin_amdgcn_mfma_f32_16x16x16bf16_1k(__builtin_bit_cast(s16x4, a), __builtin_bit_cast(s16x4, b), c, 0, 0, 0);
}
DEV float dot2(u32 a, u32 b, float c) { return __builtin_amdgcn_fdot2_f32_bf16(__builtin_bit_cast(bf16x2, a), __builtin_bit_cast(bf16x2, b), c, false); }
typedef __amdgpu_buffer_rsrc_t rsrc_t;
DEV rsrc_t mk_rsrc(const void* p, u32 bytes) { return __builtin_amdgcn_make_buffer_rsrc((void*)p, (short)0, (int)bytes, 0x00020000); }
template <int AUX> DEV u32x4 ld16(rsrc_t r, u32 off) { return __builtin_amdgcn_raw_buffer_load_b128(r, off, 0, AUX); }
template <int AUX> DEV u32x2 ld8(rsrc_t r, u32 off) { return __builtin_amdgcn_raw_buffer_load_b64(r, off, 0, AUX); }
template <int AUX> DEV u32 ld4(rsrc_t r, u32 off) { return __builtin_amdgcn_raw_buffer_load_b32(r, off, 0, AUX); }
template <int AUX> DEV void st16(rsrc_t r, u32 off, u32x4 v) { __builtin_amdgcn_raw_buffer_store_b128(v, r, off, 0, AUX); }
template <int AUX> DEV void st8(rsrc_t r, u32 off, u32x2 v) { __builtin_amdgcn_raw_buffer_store_b64(v, r, off, 0, AUX); }
template <int AUX> DEV void st4(rsrc_t r, u32 off, u32 v) { __builtin_amdgcn_raw_buffer_store_b32(v, r, off, 0, AUX); }
DEV u32x4 lds_ld16(const char* s, u32 off) { return *(const LAS u32x4*)((const LAS char*)s + off); }
DEV u32x2 lds_ld8(const char* s, u32 off) { return *(const LAS u32x2*)((const LAS char*)s + off); }
DEV u32 lds_ld4(const char* s, u32 off) { return *(const LAS u32*)((const LAS char*)s + off); }
DEV void lds_st16(char* s, u32 off, u32x4 v) { *(LAS u32x4*)((LAS char*)s + off) = v; }
DEV void lds_st8(char* s, u32 off, u32x2 v) { *(LAS u32x2*)((LAS char*)s + off) = v; }
DEV void lds_st4(char* s, u32 off, u32 v) { *(LAS u32*)((LAS char*)s + off) = v; }
DEV u32x2 lds_tr(const char* s, u32 off) {
  return __builtin_bit_cast(u32x2, __builtin_amdgcn_ds_read_tr16_b64_v4i16((LAS s16x4*)((LAS char*)s + off)));
}
DEV float f_(u32 x) { return __builtin_bit_cast(float, x); }
DEV u32 u_(float x) { return __builtin_bit_cast(u32, x); }
template <int CTRL> DEV float dpp_add(float v) {
  return v + __builtin_bit_cast(float, __builtin_amdgcn_update_dpp(0, __builtin_bit_cast(int, v), CTRL, 0xf, 0xf, true));
}
DEV float row_sum16(float v) { v = dpp_add<0xB1>(v); v = dpp_add<0x4E>(v); v = dpp_add<0x141>(v); return dpp_add<0x140>(v); }
DEV float lane_of(float v, int l) { return __builtin_bit_cast(float, __builtin_amdgcn_readlane(__builtin_bit_cast(int, v), l)); }
DEV float xor16(float v) { return __shfl_xor(v, 16); }
DEV float xor32(float v) { return __shfl_xor(v, 32); }
DEV void bar() { asm volatile("s_waitcnt lgkmcnt(0)\n\ts_barrier" ::: "memory"); }
DEV int otid() { int t = threadIdx.x; asm volatile("" : "+v"(t)); return t; }
DEV void sfence() { __builtin_amdgcn_sched_barrier(0); }
constexpr u32 SENT = 0xffffffffu;
constexpr int SC1 = 16;
constexpr int SPIN_CAP = 1 << 14;
DEV bool isbad(u32x4 v) { return max(max(v[0], v[1]), max(v[2], v[3])) == SENT; }
DEV bool isbad(u32x2 v) { return max(v[0], v[1]) == SENT; }
DEV bool anyb(bool b) { return __builtin_amdgcn_ballot_w64(b) != 0ull; }
template <int N, class F> DEV void repoll16(rsrc_t r, F off, u32x4 (&v)[N]) {
  for (int spin = 0; spin < SPIN_CAP; ++spin) {
    u32 mask = 0;
#pragma unroll
    for (int i = 0; i < N; ++i) mask |= anyb(isbad(v[i])) ? (1u << i) : 0u;
    if (!mask) break;
    __builtin_amdgcn_s_sleep(1);
#pragma unroll
    for (int i = 0; i < N; ++i) if (mask & (1u << i)) v[i] = ld16<SC1>(r, off(i));
  }
}
template <int N, int G, class F> DEV void poll16(rsrc_t r, F off, u32x4 (&v)[N]) {
  constexpr int g = G >= 0 ? G : 0;
  if (G >= 0) {
    v[g] = ld16<SC1>(r, off(g));
    for (int spin = 0; spin < SPIN_CAP; ++spin) {
      if (!anyb(isbad(v[g]))) break;
      __builtin_amdgcn_s_sleep(1);
      v[g] = ld16<SC1>(r, off(g));
    }
  }
#pragma unroll
  for (int i = 0; i < N; ++i) if (i != G) v[i] = ld16<SC1>(r, off(i));
  repoll16<N>(r, off, v);
}
DEV int permk(int g, int j) { return j < 4 ? 4 * g + j : 16 + 4 * g + (j - 4); }
DEV void ln_stats(float sum, float sq, float& m, float& rinv) {
  float mean = sum * (1.0f / 512.0f);
  float var = fmaxf(sq * (1.0f / 512.0f) - mean * mean, 0.0f);
  m = rbf(mean);
  float den = rbf(__builtin_amdgcn_sqrtf(rbf(rbf(var) + 1e-5f)));
  rinv = __builtin_amdgcn_rcpf(den);
}
DEV f32x2 unpk(u32 w) { return f32x2{bflo(w), bfhi(w)}; }
DEV f32x2 rbf2(f32x2 v) { return unpk(pk2(v[0], v[1])); }
DEV f32x2 lo2(f32x4 v) { return f32x2{v[0], v[1]}; }
DEV f32x2 hi2(f32x4 v) { return f32x2{v[2], v[3]}; }
DEV u32 phi2(f32x2 a) {
  f32x2 t = rbf2(a), s = t * 1.4426950408889634f, p = t + 1.0f;
  f32x2 e = {__builtin_amdgcn_exp2f(s[0]), __builtin_amdgcn_exp2f(s[1])};
  f32x2 q = {t[0] > 0.0f ? p[0] : e[0], t[1] > 0.0f ? p[1] : e[1]};
  q = rbf2(q) + 1e-6f;
  return pk2(q[0], q[1]);
}
constexpr u32 KA_ROW = 528;
constexpr u32 KA_TAB = 16 * KA_ROW;
constexpr u32 KA_G = 0, KA_E = 8 * KA_TAB;
constexpr u32 KA_KC = 16 * KA_TAB;
constexpr u32 KA_XT = KA_KC + 2 * 656 * 4;
constexpr u32 KA_LDS = KA_XT + 8 * 1040;
constexpr u32 KA_WIN = 0, KA_WSTR = 2064 * 16;
constexpr u32 KA_R = 100352;
typedef f32x2 cf;
DEV cf cmul(cf a, cf b) { return f32x2{a[0], a[0]} * b + f32x2{a[1], a[1]} * f32x2{-b[1], b[0]}; }
DEV cf cexp_pow(float are, float aim, int k) {
  float s = (float)(1 << k);
  float mag = __expf(are * s);
  double rev = (double)aim * (0.15915494309189535 * (double)(1 << k));
  float fr = (float)(rev - __builtin_floor(rev));
  return f32x2{__builtin_amdgcn_cosf(fr), __builtin_amdgcn_sinf(fr)} * mag;
}
DEV void phase_A(const Params& p, rsrc_t rw, char* sm, int d0, u32x4& xv0, u32x4& s0, u32x4& s1) {
  const int tid = otid(), b = blockIdx.x, lane = tid & 63, w = tid >> 6;
  const int n = tid & 127, ch = (tid >> 7) & 1, sih = tid >> 8;
  const int d = d0 + ch;
  const u32 rlA = p.logA[d * 128 + n], rAi = p.Aim[d * 128 + n], rCr = p.Cre[d * 128 + n], rCi = p.Cim[d * 128 + n], rdt = p.logdt[d];
  const u32 tag = ld4<SC1>(rw, O_EPO) + 1u;
  sfence();
  const int rg = (b & 7) * 32 + (b >> 3);
  const int row = rg * 8 + w;
  u32x4 xv = *(const u32x4*)(p.u + (size_t)row * 256 + lane * 4);
  u32 wv[8]; u32 wdst;
  {
    int g = b * NT + tid, ln = g & 63, lq = ln >> 4;
    const unsigned short* src; int ld, ks;
    if (g < 98304) {
      int f = g >> 6, ty = f % 3, f2 = f / 3, ks8 = f2 & 7, kh = (f2 >> 3) & 1, hh = (f2 >> 4) & 3, cb = f2 >> 6;
      ks = kh * 8 + ks8; src = p.wqkv + ty * 512 + (4 * cb + hh) * 16 + (ln & 15); ld = 1536; wdst = O_WQ + (u32)g * 16;
    } else {
      int g2 = g - 98304, f = g2 >> 6; ks = f & 15; int ww = (f >> 4) & 7, cb4 = f >> 7;
      src = p.wo + 128 * cb4 + 16 * ww + (ln & 15); ld = 512; wdst = O_WO + (u32)g2 * 16;
    }
#pragma unroll
    for (int j = 0; j < 8; ++j) wv[j] = src[(size_t)(32 * ks + permk(lq, j)) * ld];
  }
  {
    const u32 g = (u32)(b * NT + tid) * 16;
    const u32x4 sv = {SENT, SENT, SENT, SENT};
    st16<SC1>(rw, O_X1 + g, sv);
    if (g < O_FLG - O_ZP) st16<SC1>(rw, O_ZP + g, sv);
    if (g < 4 * L * 8) st16<SC1>(rw, O_ST2 + g, sv);
  }
  sfence();
  const float lA = bf2f(rlA), Ai = bf2f(rAi), Cr = bf2f(rCr), Ci = bf2f(rCi), dt = rbf(expf(bf2f(rdt)));
  float are = -rbf(expf(lA)) * dt, aim = Ai * dt;
  cf C = {Cr * dt, Ci * dt};
#pragma unroll
  for (int t = 0; t < 2; ++t) {
    const int si = sih + 2 * t;
    const u32 tb = (u32)(ch * 4 + si) * KA_TAB + n * 4;
    cf v0 = cexp_pow(are, aim, si), v1 = cmul(v0, v0), v2 = cmul(v1, v1), v3 = cmul(v2, v2);
    cf e[8]; e[0] = f32x2{1.f, 0.f}; e[1] = v0; e[2] = v1; e[3] = cmul(v1, v0); e[4] = v2;
    e[5] = cmul(v2, v0); e[6] = cmul(v2, v1); e[7] = cmul(v2, e[3]);
#pragma unroll
    for (int j = 0; j < 8; ++j) {
      lds_st4(sm, KA_E + tb + j * KA_ROW, pk2(e[j][0], e[j][1]));
      cf f = cmul(e[j], v3);
      lds_st4(sm, KA_E + tb + (j + 8) * KA_ROW, pk2(f[0], f[1]));
    }
    cf g0 = cexp_pow(are, aim, 4 + si), g1 = cmul(g0, g0), g2 = cmul(g1, g1), g3 = cmul(g2, g2);
    cf h[8]; h[0] = (si == 0) ? C : cmul(C, g3);
    h[1] = cmul(h[0], g0); h[2] = cmul(h[0], g1); h[3] = cmul(h[1], g1);
#pragma unroll
    for (int j = 0; j < 4; ++j) h[4 + j] = cmul(h[j], g2);
#pragma unroll
    for (int j = 0; j < 8; ++j) {
      lds_st4(sm, KA_G + tb + j * KA_ROW, pk2(h[j][0], -h[j][1]));
      cf f = cmul(h[j], g3);
      lds_st4(sm, KA_G + tb + (j + 8) * KA_ROW, pk2(f[0], -f[1]));
    }
  }
  sfence();
  {
    float s = 0.f, q = 0.f;
#pragma unroll
    for (int j = 0; j < 4; ++j) { s = dot2(xv[j], 0x3f803f80u, s); q = dot2(xv[j], xv[j], q); }
    s = row_sum16(s); q = row_sum16(q);
    s = (lane_of(s, 0) + lane_of(s, 16)) + (lane_of(s, 32) + lane_of(s, 48));
    q = (lane_of(q, 0) + lane_of(q, 16)) + (lane_of(q, 32) + lane_of(q, 48));
    if (lane == 0) { u32x2 v = {u_(s), u_(q)}; st8<SC1>(rw, O_ST0 + row * 8, v); }
    lds_st16(sm, KA_XT + w * 1040 + lane * 16, xv);
  }
  { u32x4 v = {wv[0] | (wv[1] << 16), wv[2] | (wv[3] << 16), wv[4] | (wv[5] << 16), wv[6] | (wv[7] << 16)}; st16<SC1>(rw, wdst, v); }
  bar();
  {
    u32 v[8];
#pragma unroll
    for (int r = 0; r < 8; ++r) v[r] = *(const LAS unsigned short*)((LAS char*)sm + KA_XT + r * 1040 + tid * 2);
    u32x4 o = {v[0] | (v[1] << 16), v[2] | (v[3] << 16), v[4] | (v[5] << 16), v[6] | (v[7] << 16)};
    st16<SC1>(rw, O_X0 + (((rg >> 2) * 512 + tid) * 32 + (rg & 3) * 8) * 2, o);
  }
  sfence();
  {
    const int wc = w >> 2, si = w & 3, lq = lane >> 4, r16 = lane & 15;
    const u32 base = (u32)(wc * 4 + si) * KA_TAB + r16 * KA_ROW + lq * 16;
    f32x4 acc = {0.f, 0.f, 0.f, 0.f};
#pragma unroll
    for (int ks = 0; ks < 8; ++ks) acc = mfma32(lds_ld16(sm, KA_G + base + ks * 64), lds_ld16(sm, KA_E + base + ks * 64), acc);
#pragma unroll
    for (int r = 0; r < 4; ++r) {
      int i = 4 * lq + r;
      int idx = si == 0 ? 16 * i + r16 : 256 + (si - 1) * 128 + 16 * i + r16;
      bool ok = si == 0 || i < 8 || (si == 3 && i == 8 && r16 == 0);
      if (ok) lds_st4(sm, KA_KC + (wc * 656 + idx) * 4, u_(2.0f * acc[r]));
    }
  }
  bar();
  const int c2 = tid >> 8, m = tid & 255;
  {
    u32 o[4];
#pragma unroll
    for (int j = 0; j < 4; ++j) {
      float kv[2];
#pragma unroll
      for (int h = 0; h < 2; ++h) {
        int l = 2047 - (8 * m + 2 * j + h);
        int e = l < 256 ? 0 : (l < 512 ? 1 : (l < 1024 ? 2 : 3));
        int mm = (l + ((1 << e) >> 1) - 1 + ((l >> e) & 1)) >> e;
        int idx = e == 0 ? l : 256 + (e - 1) * 128 + (mm - 128);
        kv[h] = f_(lds_ld4(sm, KA_KC + (c2 * 656 + idx) * 4));
      }
      o[j] = pk2(kv[0], kv[1]);
    }
    lds_st16(sm, KA_R + c2 * KR_STR + m * 16, u32x4{o[0], o[1], o[2], o[3]});
    if (m < 2) lds_st16(sm, KA_R + c2 * KR_STR + 4096 + m * 16, u32x4{0u, 0u, 0u, 0u});
  }
  asm volatile("s_waitcnt vmcnt(0)" ::: "memory");
  bar();
  if (tid == 0) st4<SC1>(rw, O_FLG + b * FLG_STR, tag);
#pragma unroll
  for (int i = 0; i < 4; ++i) {
    const u32 rb_ = KA_R + c2 * KR_STR + (m + 256 * i) * 4;
    u32 a[5];
#pragma unroll
    for (int j = 0; j < 5; ++j) a[j] = lds_ld4(sm, rb_ + j * 4);
    const u32 gb = KA_WIN + c2 * KA_WSTR + (m + 256 * i) * 32;
    lds_st16(sm, gb, u32x4{a[0], a[1], a[2], a[3]});
    lds_st16(sm, gb + 16, u32x4{__builtin_amdgcn_alignbit(a[1], a[0], 16), __builtin_amdgcn_alignbit(a[2], a[1], 16),
                                __builtin_amdgcn_alignbit(a[3], a[2], 16), __builtin_amdgcn_alignbit(a[4], a[3], 16)});
  }
  if (m < 16) lds_st16(sm, KA_WIN + c2 * KA_WSTR + (2048 + m) * 16, u32x4{0u, 0u, 0u, 0u});
  if (w < 4) {
    u32 fl = ld4<SC1>(rw, O_FLG + tid * FLG_STR);
    for (int spin = 0; spin < SPIN_CAP; ++spin) {
      if (!anyb(fl != tag)) break;
      __builtin_amdgcn_s_sleep(1);
      fl = ld4<SC1>(rw, O_FLG + tid * FLG_STR);
    }
  }
  bar();
  if (b == 0 && tid == 0) st4<SC1>(rw, O_EPO, tag);
  xv0 = ld16<SC1>(rw, O_X0 + (((m >> 2) * 512 + d0 + c2) * 32 + (m & 3) * 8) * 2);
  s0 = ld16<SC1>(rw, O_ST0 + tid * 32); s1 = ld16<SC1>(rw, O_ST0 + tid * 32 + 16);
}
constexpr u32 P_G = 0, P_GSTR = 2064 * 16;
constexpr u32 DB = 2 * P_GSTR;
constexpr u32 P_CS = 158720;
constexpr u32 P_LDS = P_CS + 8 * 3 * 16 * 4;
constexpr u32 C_U = DB, C_ST = C_U + 2 * 4608, C_RED = C_ST + L * 8;
constexpr u32 Q_A = DB, Q_EX = DB, Q_SP = DB + 65536, Q_MR = Q_SP + 4096;
constexpr u32 O_OF = DB, O_T = DB + 32768, O_PS = O_T + 128 * 144;
constexpr u32 R_B = DB;
static_assert(KA_LDS <= P_CS && KA_WIN == P_G && KA_WSTR == P_GSTR && KA_R + 2 * KR_STR <= KA_KC && KA_R >= C_RED && C_RED + 65536 <= P_CS && Q_MR + 8 * 512 <= P_CS && P_LDS <= 163840, "LDS map");


DEV void fill16(rsrc_t rw, u32 off) { st16<SC1>(rw, off, u32x4{SENT, SENT, SENT, SENT}); }
template <int NP, bool POLL>
DEV void phase_C(rsrc_t rw, char* sm, u32x4 xv0, u32x4 s0, u32x4 s1, u32x4 (&W)[8][3], int cb, int d0, float dsk, u32 o_in, u32 o_st, u32 o_out, int sb) {
  const int tid = otid(), lane = tid & 63, w = tid >> 6, ch = tid >> 8, m = tid & 255;
  const u32 xoff = (((m >> 2) * 512 + d0 + ch) * 32 + (m & 3) * 8) * 2;
  u32x4 xv;
  {
    u32x4 sv[2 * NP + 1];
    auto off = [&](int i) -> u32 { return i == 2 * NP ? o_in + xoff : o_st + (i >> 1) * L * 8 + tid * 32 + (i & 1) * 16; };
    if (POLL) poll16<2 * NP + 1, (POLL ? 2 * NP : -1)>(rw, off, sv);
    else {
      sv[0] = s0; sv[1] = s1; sv[2 * NP] = xv0;
      const u32 fg = (u32)(blockIdx.x * NT + tid) * 16;
      fill16(rw, O_PQ + fg); fill16(rw, O_KVP + fg);
    }
    xv = sv[2 * NP];
    float mr[8];
#pragma unroll
    for (int r = 0; r < 4; ++r) {
      float s = 0.f, q2 = 0.f;
#pragma unroll
      for (int q = 0; q < NP; ++q) { s += f_(sv[2 * q + (r >> 1)][(r & 1) * 2]); q2 += f_(sv[2 * q + (r >> 1)][(r & 1) * 2 + 1]); }
      ln_stats(s, q2, mr[2 * r], mr[2 * r + 1]);
    }
    lds_st16(sm, C_ST + tid * 32, u32x4{u_(mr[0]), u_(mr[2]), u_(mr[1]), u_(mr[3])});
    lds_st16(sm, C_ST + tid * 32 + 16, u32x4{u_(mr[4]), u_(mr[6]), u_(mr[5]), u_(mr[7])});
    if (tid < 64) lds_st16(sm, C_U + (tid >> 5) * 4608 + ((tid >> 4) & 1) * 2304 + (tid & 15) * 16, u32x4{0u, 0u, 0u, 0u});
  }
  asm volatile("s_waitcnt vmcnt(0)" ::: "memory");
  bar();
  u32x4 uv;
#pragma unroll
  for (int j = 0; j < 4; ++j) {
    u32x4 s = lds_ld16(sm, C_ST + (8 * m + 2 * j) * 8);
    f32x2 t = rbf2(unpk(xv[j]) - f32x2{f_(s[0]), f_(s[1])}) * f32x2{f_(s[2]), f_(s[3])};
    uv[j] = pk2(t[0], t[1]);
  }
  lds_st16(sm, C_U + ch * 4608 + (m & 1) * 2304 + (16 + (m >> 1)) * 16, uv);
  bar();
  {
    const int wc = w >> 2, w4 = w & 3, lq = lane >> 4, r16 = lane & 15;
    const int j0 = (lq & 1) * 16 + (lq >> 1) * 8, rho = 2 * w4;
    const u32 ub = C_U + wc * 4608 + (lq >> 1) * 2304 + (15 - 2 * rho + r16 + (lq & 1)) * 16;
    const u32 gb = P_G + wc * P_GSTR + (2031 - 32 * (rho + 1) - 256 * 7 - r16 + j0) * 16;
    f32x4 acc[8];
#pragma unroll
    for (int c = 0; c < 8; ++c) acc[c] = f32x4{0.f, 0.f, 0.f, 0.f};
    u32x4 B0[8], A0[8], B1[8], A1[8];
#pragma unroll
    for (int k = 0; k < 8; ++k) { if (k == 0) A0[0] = lds_ld16(sm, gb + 512 + 7 * 4096); B0[7 - k] = lds_ld16(sm, ub + (7 - k) * 256); }
#pragma unroll
    for (int k = 1; k < 8; ++k) A0[k] = lds_ld16(sm, gb + 512 + (7 - k) * 4096);
    sfence();
    const u32 wb = O_WQ + (u32)(cb * 8 + (w & 3) * 2 + (w >> 2)) * 24576 + lane * 16;
#pragma unroll
    for (int g = 0; g < 8; ++g) {
      A1[g] = lds_ld16(sm, gb + (7 - g) * 4096); B1[g] = lds_ld16(sm, ub - 32 + g * 256);
#pragma unroll
      for (int i = 2 * g; i < 2 * g + 2; ++i) W[i / 3][i % 3] = ld16<SC1>(rw, wb + i * 1024);
      sfence();
#pragma unroll
      for (int c = 7; c >= g; --c) acc[c] = mfma32(A0[g], B0[c - g], acc[c]);
      sfence();
    }
#pragma unroll
    for (int g = 0; g < 8; ++g) {
      W[(16 + g) / 3][(16 + g) % 3] = ld16<SC1>(rw, wb + (16 + g) * 1024);
      sfence();
#pragma unroll
      for (int c = g; c < 8; ++c) acc[c] = mfma32(A1[g], B1[c - g], acc[c]);
      sfence();
    }
#pragma unroll
    for (int c = 0; c < 8; ++c) lds_st16(sm, C_RED + ((wc * 4 + w4) * 8 + c) * 1024 + lane * 16, __builtin_bit_cast(u32x4, acc[c]));
  }
  bar();
  {
    const int c = m >> 5, a = (m & 31) >> 1, lq0 = (m & 1) * 2;
    f32x4 y0 = {0.f, 0.f, 0.f, 0.f}, y1 = y0;
#pragma unroll
    for (int q = 0; q < 4; ++q) {
      const u32 o = C_RED + ((ch * 4 + q) * 8 + c) * 1024 + (lq0 * 16 + a) * 16;
      y0 += __builtin_bit_cast(f32x4, lds_ld16(sm, o)); y1 += __builtin_bit_cast(f32x4, lds_ld16(sm, o + 256));
    }
    u32x4 ov;
#pragma unroll
    for (int j = 0; j < 4; ++j) {
      f32x2 y = j == 0 ? lo2(y0) : j == 1 ? hi2(y0) : j == 2 ? lo2(y1) : hi2(y1);
      f32x2 r = unpk(xv[j]) + rbf2(rbf2(y) + rbf2(unpk(uv[j]) * dsk));
      ov[j] = pk2(r[0], r[1]);
    }
    st16<SC1>(rw, o_out + xoff, ov);
  }
  bar();
}

DEV void q_colsum(char* sm, const u32x4 (&W)[8][3]) {
  const int tid = otid(), lane = tid & 63, w = tid >> 6;
#pragma unroll
  for (int ty = 0; ty < 3; ++ty) {
    float s = 0.f;
#pragma unroll
    for (int ks = 0; ks < 8; ++ks)
#pragma unroll
      for (int j = 0; j < 4; ++j) s = dot2(W[ks][ty][j], 0x3f803f80u, s);
    s += xor16(s); s += xor32(s);
    if (lane < 16) lds_st4(sm, P_CS + ((((w & 3) * 2 + (w >> 2)) * 3 + ty) * 16 + lane) * 4, u_(s));
  }
}
template <bool FIRST>
DEV void phase_Q(rsrc_t rw, char* sm, const u32x4 (&W)[8][3], u32x4 (&Wo)[16], int rb, int cb, int cb4, u32 o_x, u32 o_pq, u32 o_kvp, u32 o_zp,
                 int sb) {
  const int tid = otid(), lane = tid & 63, w = tid >> 6, hh = w & 3, kh = w >> 2, lq = lane >> 4, r16 = lane & 15;
  {
    u32x4 av[8];
    poll16<8, 0>(rw, [&](int i) -> u32 { return o_x + (u32)rb * 65536 + (tid + i * 512) * 16; }, av);
    const int chn = tid >> 2, pc = tid & 3, f = (chn >> 1) & 3;
#pragma unroll
    for (int i = 0; i < 8; ++i)
      lds_st16(sm, Q_A + (chn + 128 * (i & 3)) * 128 + ((((i >> 2) * 2 + (pc >> 1)) ^ f) * 32) + (pc & 1) * 16, av[i]);
  }
  bar();
  f32x4 acc[4][3];
#pragma unroll
  for (int rt = 0; rt < 4; ++rt)
#pragma unroll
    for (int ty = 0; ty < 3; ++ty) acc[rt][ty] = f32x4{0.f, 0.f, 0.f, 0.f};
  const int chl = 4 * lq + (r16 >> 2), f = (chl >> 1) & 3;
  u32 ab[4];
#pragma unroll
  for (int rt = 0; rt < 4; ++rt) ab[rt] = Q_A + (kh * 256 + chl) * 128 + ((rt ^ f) * 32) + (r16 & 3) * 8;
  const u32 abs_ = Q_A + (kh * 256 + chl) * 128 + ((hh ^ f) * 32) + (r16 & 3) * 8;
  float ssum = 0.f, ssq = 0.f;
  const u32 wob = O_WO + (u32)(cb4 * 8 + w) * 16384 + lane * 16;
  {
    u32x2 xa[2][4][2], xs[2][2];
#pragma unroll
    for (int rt = 0; rt < 4; ++rt) { xa[0][rt][0] = lds_tr(sm, ab[rt]); xa[0][rt][1] = lds_tr(sm, ab[rt] + 2048); }
    xs[0][0] = lds_tr(sm, abs_); xs[0][1] = lds_tr(sm, abs_ + 2048);
#pragma unroll
    for (int ks = 0; ks < 8; ++ks) {
      const int cu = ks & 1, nx = cu ^ 1;
      if (ks < 7) {
#pragma unroll
        for (int rt = 0; rt < 4; ++rt) { xa[nx][rt][0] = lds_tr(sm, ab[rt] + (ks + 1) * 4096); xa[nx][rt][1] = lds_tr(sm, ab[rt] + (ks + 1) * 4096 + 2048); }
        xs[nx][0] = lds_tr(sm, abs_ + (ks + 1) * 4096); xs[nx][1] = lds_tr(sm, abs_ + (ks + 1) * 4096 + 2048);
      }
      Wo[2 * ks] = ld16<SC1>(rw, wob + 2 * ks * 1024); Wo[2 * ks + 1] = ld16<SC1>(rw, wob + (2 * ks + 1) * 1024);
      if (FIRST && ks == 1) fill16(rw, O_X2 + (u32)(blockIdx.x * NT + tid) * 16);
      if (FIRST && ks == 4) fill16(rw, O_X3 + (u32)(blockIdx.x * NT + tid) * 16);
      sfence();
#pragma unroll
      for (int rt = 0; rt < 4; ++rt) {
        u32x4 X = {xa[cu][rt][0][0], xa[cu][rt][0][1], xa[cu][rt][1][0], xa[cu][rt][1][1]};
        acc[rt][0] = mfma32(W[ks][0], X, acc[rt][0]);
        acc[rt][1] = mfma32(X, W[ks][1], acc[rt][1]);
        acc[rt][2] = mfma32(X, W[ks][2], acc[rt][2]);
      }
#pragma unroll
      for (int j = 0; j < 2; ++j)
#pragma unroll
        for (int e = 0; e < 2; ++e) { ssum = dot2(xs[cu][j][e], 0x3f803f80u, ssum); ssq = dot2(xs[cu][j][e], xs[cu][j][e], ssq); }
      sfence();
    }
  }
  lds_st8(sm, Q_SP + ((kh * 4 + lq) * 64 + hh * 16 + r16) * 8, u32x2{u_(ssum), u_(ssq)});
  bar();
  const u32 ex = Q_EX + hh * 12288 + lane * 16;
  if (kh == 0) {
#pragma unroll
    for (int rt = 0; rt < 4; ++rt) lds_st16(sm, ex + rt * 1024, __builtin_bit_cast(u32x4, acc[rt][0]));
  } else {
#pragma unroll
    for (int rt = 0; rt < 4; ++rt) {
      lds_st16(sm, ex + (4 + rt * 2) * 1024, __builtin_bit_cast(u32x4, acc[rt][1]));
      lds_st16(sm, ex + (5 + rt * 2) * 1024, __builtin_bit_cast(u32x4, acc[rt][2]));
    }
  }
  {
    float sum = 0.f, sq = 0.f;
#pragma unroll
    for (int i = 0; i < 8; ++i) { u32x2 pp = lds_ld8(sm, Q_SP + (i * 64 + lane) * 8); sum += f_(pp[0]); sq += f_(pp[1]); }
    float mean, rinv; ln_stats(sum, sq, mean, rinv);
    lds_st4(sm, Q_MR + w * 512 + lane * 4, u_(mean)); lds_st4(sm, Q_MR + w * 512 + 256 + lane * 4, u_(rinv));
  }
  bar();
  const int h = cb * 4 + hh;
  const u32 mrb = Q_MR + w * 512, csb = P_CS + hh * 2 * 3 * 16 * 4;
  if (kh == 1) {
    const f32x4 cs = __builtin_bit_cast(f32x4, lds_ld16(sm, csb + 16 * lq)) + __builtin_bit_cast(f32x4, lds_ld16(sm, csb + 192 + 16 * lq));
#pragma unroll
    for (int rp = 0; rp < 2; ++rp) {
      u32x4 ov;
#pragma unroll
      for (int t = 0; t < 2; ++t) {
        const int rt = rp * 2 + t;
        f32x4 a = acc[rt][0] + __builtin_bit_cast(f32x4, lds_ld16(sm, ex + rt * 1024));
        const float mean = f_(lds_ld4(sm, mrb + (rt * 16 + r16) * 4)), rinv = f_(lds_ld4(sm, mrb + 256 + (rt * 16 + r16) * 4));
        ov[2 * t] = phi2((lo2(a) - lo2(cs) * mean) * rinv); ov[2 * t + 1] = phi2((hi2(a) - hi2(cs) * mean) * rinv);
      }
      st16<SC1>(rw, o_pq + (((rb * 2 + rp) * 32 + h) * 64 + lane) * 16, ov);
    }
  } else {
    const float csk = f_(lds_ld4(sm, csb + 64 + r16 * 4)) + f_(lds_ld4(sm, csb + 192 + 64 + r16 * 4));
    const float csv = f_(lds_ld4(sm, csb + 128 + r16 * 4)) + f_(lds_ld4(sm, csb + 192 + 128 + r16 * 4));
    f32x4 kv = {0.f, 0.f, 0.f, 0.f}, zz = kv;
#pragma unroll
    for (int rp = 0; rp < 2; ++rp) {
      u32x4 pk, vv;
#pragma unroll
      for (int t = 0; t < 2; ++t) {
        const int rt = rp * 2 + t;
        f32x4 ak = acc[rt][1] + __builtin_bit_cast(f32x4, lds_ld16(sm, ex + (4 + rt * 2) * 1024));
        f32x4 av = acc[rt][2] + __builtin_bit_cast(f32x4, lds_ld16(sm, ex + (5 + rt * 2) * 1024));
        const f32x4 mean = __builtin_bit_cast(f32x4, lds_ld16(sm, mrb + (rt * 16 + 4 * lq) * 4));
        const f32x4 rinv = __builtin_bit_cast(f32x4, lds_ld16(sm, mrb + 256 + (rt * 16 + 4 * lq) * 4));
        pk[2 * t] = phi2((lo2(ak) - lo2(mean) * csk) * lo2(rinv)); pk[2 * t + 1] = phi2((hi2(ak) - hi2(mean) * csk) * hi2(rinv));
        f32x2 v0 = (lo2(av) - lo2(mean) * csv) * lo2(rinv), v1 = (hi2(av) - hi2(mean) * csv) * hi2(rinv);
        vv[2 * t] = pk2(v0[0], v0[1]); vv[2 * t + 1] = pk2(v1[0], v1[1]);
      }
      kv = mfma32(pk, vv, kv);
      zz = mfma32(pk, u32x4{0x3f803f80u, 0x3f803f80u, 0x3f803f80u, 0x3f803f80u}, zz);
    }
    st16<SC1>(rw, o_kvp + ((rb * 32 + h) * 64 + lane) * 16, __builtin_bit_cast(u32x4, kv));
    if (r16 == 0) st16<SC1>(rw, o_zp + (rb * 32 + h) * 64 + lq * 16, __builtin_bit_cast(u32x4, zz));
  }
  bar();
}

DEV void phase_R(rsrc_t rw, char* sm, int b, u32 o_kvp, u32 o_zp, u32 o_kvt, u32 o_zt) {
  const int tid = otid(), h = b >> 3, j = b & 7, rb = tid >> 4, q = tid & 15;
  u32x4 v[1];
  const u32 off = q < 8 ? o_kvp + ((rb * 32 + h) * 64 + 8 * j + q) * 16 : o_zp + (rb * 32 + h) * 64 + (j >> 1) * 16;
  poll16<1, -1>(rw, [&](int) -> u32 { return off; }, v);
  asm volatile("s_waitcnt vmcnt(0)" ::: "memory");
  if (q < 8) lds_st16(sm, R_B + (rb * 40 + q * 4) * 4, v[0]);
  else if (q == 8) lds_st8(sm, R_B + (rb * 40 + 32) * 4, (j & 1) ? u32x2{v[0][2], v[0][3]} : u32x2{v[0][0], v[0][1]});
  bar();
  if (tid < 64) {
    const int e = tid < 34 ? tid : 0;
    float s = 0.f;
#pragma unroll
    for (int r = 0; r < 32; ++r) s += f_(lds_ld4(sm, R_B + (r * 40 + e) * 4));
    float o = __shfl_down(s, 1), o2 = __shfl_down(s, 2), o3 = __shfl_down(s, 3);
    if (tid < 32 && (tid & 3) == 0) st8<SC1>(rw, o_kvt + (h * 64 + 8 * j + (tid >> 2)) * 8, u32x2{pk2(s, o), pk2(o2, o3)});
    if (tid == 32) st4<SC1>(rw, o_zt + h * 32 + j * 4, pk2(s, o));
  }
  bar();
}

DEV void o_prefetch(rsrc_t rw, u32x4 (&v)[5], int rb, int cb4, u32 o_pq, u32 o_xin) {
  const int tid = otid(), lane = tid & 63, w = tid >> 6;
#pragma unroll
  for (int i = 0; i < 4; ++i) v[i] = ld16<SC1>(rw, o_pq + ((rb * 32 + 4 * w + i) * 64 + lane) * 16);
  v[4] = ld16<SC1>(rw, o_xin + ((rb * 512 + cb4 * 128 + (tid >> 2)) * 32 + (tid & 3) * 8) * 2);
}
template <bool FINAL>
DEV void phase_O(unsigned short* out, rsrc_t rw, char* sm, const u32x4 (&Wo)[16], u32x4 (&v)[5], int rb, int cb4, u32 o_pq, u32 o_kvt, u32 o_zt,
                 u32 o_xin, u32 o_xout, u32 o_st, int sb) {
  const int tid = otid(), lane = tid & 63, w = tid >> 6, lq = lane >> 4, r16 = lane & 15;
  const int ecol = tid >> 2, epc = tid & 3;
  const u32 xoff = ((rb * 512 + cb4 * 128 + ecol) * 32 + epc * 8) * 2;
  u32x4 xin;
  {
    u32x2 kv[4], zt[4];
    kv[3] = ld8<SC1>(rw, o_kvt + ((4 * w + 3) * 64 + lane) * 8);
    for (int spin = 0; spin < SPIN_CAP; ++spin) {
      if (!anyb(isbad(kv[3]))) break;
      __builtin_amdgcn_s_sleep(1);
      kv[3] = ld8<SC1>(rw, o_kvt + ((4 * w + 3) * 64 + lane) * 8);
    }
#pragma unroll
    for (int i = 0; i < 4; ++i) { if (i < 3) kv[i] = ld8<SC1>(rw, o_kvt + ((4 * w + i) * 64 + lane) * 8); zt[i] = ld8<SC1>(rw, o_zt + (4 * w + i) * 32 + lq * 8); }
    repoll16<5>(rw, [&](int i) -> u32 { return i < 4 ? o_pq + ((rb * 32 + 4 * w + i) * 64 + lane) * 16 : o_xin + xoff; }, v);
    xin = v[4];
    for (int spin = 0; spin < SPIN_CAP; ++spin) {
      bool bad = false;
#pragma unroll
      for (int i = 0; i < 4; ++i) bad = bad || isbad(kv[i]) || isbad(zt[i]);
      if (!anyb(bad)) break;
      __builtin_amdgcn_s_sleep(1);
#pragma unroll
      for (int i = 0; i < 4; ++i) { kv[i] = ld8<SC1>(rw, o_kvt + ((4 * w + i) * 64 + lane) * 8); zt[i] = ld8<SC1>(rw, o_zt + (4 * w + i) * 32 + lq * 8); }
    }
#pragma unroll
    for (int hp = 0; hp < 2; ++hp)
#pragma unroll
      for (int rt = 0; rt < 2; ++rt) {
        u32x4 of;
#pragma unroll
        for (int t = 0; t < 2; ++t) {
          const int i = hp * 2 + t;
          u32x2 B = {v[i][2 * rt], v[i][2 * rt + 1]};
          f32x4 z4 = {0.f, 0.f, 0.f, 0.f};
          f32x4 num = mfma16(kv[i], B, z4), den = mfma16(zt[i], B, z4);
          const float dr = __builtin_amdgcn_rcpf(rbf(rbf(den[0]) + 1e-6f));
          f32x2 o0 = rbf2(lo2(num)) * dr, o1 = rbf2(hi2(num)) * dr;
          of[2 * t] = pk2(o0[0], o0[1]); of[2 * t + 1] = pk2(o1[0], o1[1]);
        }
        lds_st16(sm, O_OF + ((rt * 16 + 2 * w + hp) * 64 + lane) * 16, of);
      }
  }
  bar();
  {
    f32x4 a0 = {0.f, 0.f, 0.f, 0.f}, a1 = a0;
    u32x4 o0[16], o1[16];
#pragma unroll
    for (int ks = 0; ks < 4; ++ks) { o0[ks] = lds_ld16(sm, O_OF + (ks * 64 + lane) * 16); o1[ks] = lds_ld16(sm, O_OF + ((16 + ks) * 64 + lane) * 16); }
#pragma unroll
    for (int ks = 0; ks < 16; ++ks) {
      if (!FINAL && ks == 2) fill16(rw, O_PQ + XSZ + (u32)(blockIdx.x * NT + tid) * 16);
      if (ks + 4 < 16) { o0[ks + 4] = lds_ld16(sm, O_OF + ((ks + 4) * 64 + lane) * 16); o1[ks + 4] = lds_ld16(sm, O_OF + ((20 + ks) * 64 + lane) * 16); }
      sfence();
      a0 = mfma32(o0[ks], Wo[ks], a0);
      a1 = mfma32(o1[ks], Wo[ks], a1);
      sfence();
    }
    lds_st16(sm, O_T + (16 * w + r16) * 144 + (4 * lq) * 4, __builtin_bit_cast(u32x4, a0));
    lds_st16(sm, O_T + (16 * w + r16) * 144 + (16 + 4 * lq) * 4, __builtin_bit_cast(u32x4, a1));
  }
  bar();
  f32x4 t0 = __builtin_bit_cast(f32x4, lds_ld16(sm, O_T + ecol * 144 + epc * 32)), t1 = __builtin_bit_cast(f32x4, lds_ld16(sm, O_T + ecol * 144 + epc * 32 + 16));
  u32x4 ov;
#pragma unroll
  for (int j = 0; j < 4; ++j) {
    f32x2 r = unpk(xin[j]) + rbf2(j == 0 ? lo2(t0) : j == 1 ? hi2(t0) : j == 2 ? lo2(t1) : hi2(t1));
    ov[j] = pk2(r[0], r[1]);
  }
  if (!FINAL) st16<SC1>(rw, o_xout + xoff, ov);
  lds_st16(sm, O_OF + ecol * 80 + epc * 16, ov);
  bar();
  const int rt = w & 1, cq = w >> 1;
  const u32 tb = O_OF + (cq * 32 + 8 * lq + (r16 >> 2)) * 80 + rt * 32 + (r16 & 3) * 8;
  const u32x2 c0 = lds_tr(sm, tb), c1 = lds_tr(sm, tb + 4 * 80);
  if (!FINAL) {
    float s = dot2(c0[0], 0x3f803f80u, 0.f), q = dot2(c0[0], c0[0], 0.f);
    s = dot2(c0[1], 0x3f803f80u, s); q = dot2(c0[1], c0[1], q);
    s = dot2(c1[0], 0x3f803f80u, s); q = dot2(c1[0], c1[0], q);
    s = dot2(c1[1], 0x3f803f80u, s); q = dot2(c1[1], c1[1], q);
    lds_st8(sm, O_PS + ((cq * 4 + lq) * 32 + rt * 16 + r16) * 8, u32x2{u_(s), u_(q)});
    bar();
    if (tid < 32) {
      float sum = 0.f, sq = 0.f;
#pragma unroll
      for (int i = 0; i < 16; ++i) { u32x2 pp = lds_ld8(sm, O_PS + (i * 32 + tid) * 8); sum += f_(pp[0]); sq += f_(pp[1]); }
      st8<SC1>(rw, o_st + (cb4 * L + rb * 32 + tid) * 8, u32x2{u_(sum), u_(sq)});
    }
    bar();
  } else {
    *(u32x4*)(out + (size_t)(rb * 32 + rt * 16 + r16) * 512 + cb4 * 128 + cq * 32 + lq * 8) = u32x4{c0[0], c0[1], c1[0], c1[1]};
  }
}

extern __shared__ __attribute__((aligned(16))) char smem[];
__global__ void __launch_bounds__(512, 2) kB(Params p) {
  rsrc_t rw = mk_rsrc(p.ws, O_END);
  const int b = blockIdx.x, tid = otid();
  const int d0 = 2 * ((b & 7) * 32 + (b >> 3));
  const int rbq = (b & 7) * 4 + ((b >> 3) & 3), cb = b >> 5;
  const int rbo = (b & 7) * 8 + ((b >> 3) & 7), cb4 = b >> 6;
  const float dsk = bf2f(p.Dskip[d0 + (tid >> 8)]);
  u32 pcv;
  {
    unsigned long long pc; u32 len;
    asm volatile("s_getpc_b64 %0\nkb_text_begin:\n\ts_mov_b32 %1, kb_text_end-kb_text_begin" : "=s"(pc), "=s"(len));
    const u32 o = ((u32)(b >> 3) * NT + tid) * 4;
    pcv = *(const __attribute__((address_space(1))) u32*)(pc + min(o, len - 4u));
  }
  u32x4 W[8][3], Wo[16], ov5[5], xv0, s0, s1;
  phase_A(p, rw, smem, d0, xv0, s0, s1);
  asm volatile("" :: "v"(pcv));
  phase_C<1, false>(rw, smem, xv0, s0, s1, W, cb, d0, dsk, O_X0, O_ST0, O_X1, 3);
  q_colsum(smem, W);
  phase_Q<true>(rw, smem, W, Wo, rbq, cb, cb4, O_X1, O_PQ, O_KVP, O_ZP, 6);
  phase_R(rw, smem, b, O_KVP, O_ZP, O_KVT, O_ZT);
  o_prefetch(rw, ov5, rbo, cb4, O_PQ, O_X1);
  phase_O<false>(p.out, rw, smem, Wo, ov5, rbo, cb4, O_PQ, O_KVT, O_ZT, O_X1, O_X2, O_ST2, 10);
  phase_C<4, true>(rw, smem, xv0, s0, s1, W, cb, d0, dsk, O_X2, O_ST2, O_X3, 13);
  phase_Q<false>(rw, smem, W, Wo, rbq, cb, cb4, O_X3, O_PQ + XSZ, O_KVP + KVP_SZ, O_ZP + ZP_SZ, 16);
  phase_R(rw, smem, b, O_KVP + KVP_SZ, O_ZP + ZP_SZ, O_KVT + KVT_SZ, O_ZT + ZT_SZ);
  o_prefetch(rw, ov5, rbo, cb4, O_PQ + XSZ, O_X3);
  phase_O<true>(p.out, rw, smem, Wo, ov5, rbo, cb4, O_PQ + XSZ, O_KVT + KVT_SZ, O_ZT + ZT_SZ, O_X3, 0u, 0u, 20);
  asm volatile("kb_text_end:");
}
static Params make_params(void* const* d_in, void* d_out, void* d_ws) {
  Params p;
  p.u = (const u32*)d_in[0]; p.logA = (const unsigned short*)d_in[1]; p.Aim = (const unsigned short*)d_in[2];
  p.Cre = (const unsigned short*)d_in[3]; p.Cim = (const unsigned short*)d_in[4]; p.logdt = (const unsigned short*)d_in[5];
  p.Dskip = (const unsigned short*)d_in[6]; p.wqkv = (const unsigned short*)d_in[8]; p.wo = (const unsigned short*)d_in[9];
  p.ws = (char*)d_ws; p.out = (unsigned short*)d_out;
  return p;
}
extern "C" void kernel_launch(void* const* d_in, const int* in_sizes, int n_in, void* d_out, int out_size, void* d_ws, size_t ws_size,
                              hipStream_t stream) {
  Params p = make_params(d_in, d_out, d_ws);
  (void)hipFuncSetAttribute((const void*)kB, hipFuncAttributeMaxDynamicSharedMemorySize, P_LDS);
  kB<<<NB, NT, P_LDS, stream>>>(p);
}
```

```cpp
#include <hip/hip_runtime.h>
#include <hip/hip_bf16.h>
#include <stdint.h>

typedef unsigned u32;
typedef unsigned u32x4 __attribute__((ext_vector_type(4)));
typedef unsigned u32x2 __attribute__((ext_vector_type(2)));
typedef float f32x4 __attribute__((ext_vector_type(4)));
typedef float f32x2 __attribute__((ext_vector_type(2)));
typedef short s16x4 __attribute__((ext_vector_type(4)));
typedef __bf16 bf16x8 __attribute__((ext_vector_type(8)));
typedef __bf16 bf16x2 __attribute__((ext_vector_type(2)));
#define LAS __attribute__((address_space(3)))
#define DEV __device__ __forceinline__

constexpr int L = 2048, D = 512, NB = 256, NT = 512;
constexpr u32 O_WQ = 0;
constexpr u32 O_WO = O_WQ + 1536 * 512 * 2;
constexpr u32 KR_STR = 2064 * 2;
constexpr u32 XSZ = L * D * 2;
constexpr u32 O_X0 = O_WO + 512 * 512 * 2;
constexpr u32 O_ST0 = O_X0 + XSZ;
constexpr u32 O_X1 = O_ST0 + L * 8;
constexpr u32 O_X2 = O_X1 + XSZ;
constexpr u32 O_X3 = O_X2 + XSZ;
constexpr u32 O_ST2 = O_X3 + XSZ;
constexpr u32 O_PQ = O_ST2 + 4 * L * 8;
constexpr u32 O_KVP = O_PQ + 2 * XSZ;
constexpr u32 KVP_SZ = 32 * 32 * 1024;
constexpr u32 O_ZP = O_KVP + 2 * KVP_SZ;
constexpr u32 ZP_SZ = 32 * 32 * 64;
constexpr u32 O_KVT = O_ZP + 2 * ZP_SZ;
constexpr u32 KVT_SZ = 32 * 512;
constexpr u32 O_ZT = O_KVT + 2 * KVT_SZ;
constexpr u32 ZT_SZ = 32 * 32;
constexpr u32 FLG_STR = 64;
constexpr u32 O_FLG = O_ZT + 2 * ZT_SZ;
constexpr u32 O_EPO = O_FLG + 256 * FLG_STR;
constexpr u32 O_END = O_EPO + 128;

struct Params {
  const u32* u; const unsigned short* logA; const unsigned short* Aim; const unsigned short* Cre; const unsigned short* Cim;
  const unsigned short* logdt; const unsigned short* Dskip; const unsigned short* wqkv; const unsigned short* wo;
  char* ws; unsigned short* out;
};

DEV float bf2f(u32 h) { return __builtin_bit_cast(float, h << 16); }
DEV float bflo(u32 w) { return __builtin_bit_cast(float, w << 16); }
DEV float bfhi(u32 w) { return __builtin_bit_cast(float, w & 0xffff0000u); }
DEV u32 pk2(float lo, float hi) { f32x2 f = {lo, hi}; return __builtin_bit_cast(u32, __builtin_convertvector(f, bf16x2)); }
DEV float rbf(float x) { return bflo(pk2(x, x)); }
DEV f32x4 mfma32(u32x4 a, u32x4 b, f32x4 c) {
  return __builtin_amdgcn_mfma_f32_16x16x32_bf16(__builtin_bit_cast(bf16x8, a), __builtin_bit_cast(bf16x8, b), c, 0, 0, 0);
}
DEV f32x4 mfma16(u32x2 a, u32x2 b, f32x4 c) {
  return __builtin_amdgcn_mfma_f32_16x16x16bf16_1k(__builtin_bit_cast(s16x4, a), __builtin_bit_cast(s16x4, b), c, 0, 0, 0);
}
DEV float dot2(u32 a, u32 b, float c) { return __builtin_amdgcn_fdot2_f32_bf16(__builtin_bit_cast(bf16x2, a), __builtin_bit_cast(bf16x2, b), c, false); }
typedef __amdgpu_buffer_rsrc_t rsrc_t;
DEV rsrc_t mk_rsrc(const void* p, u32 bytes) { return __builtin_amdgcn_make_buffer_rsrc((void*)p, (short)0, (int)bytes, 0x00020000); }
template <int AUX> DEV u32x4 ld16(rsrc_t r, u32 off) { return __builtin_amdgcn_raw_buffer_load_b128(r, off, 0, AUX); }
template <int AUX> DEV u32x2 ld8(rsrc_t r, u32 off) { return __builtin_amdgcn_raw_buffer_load_b64(r, off, 0, AUX); }
template <int AUX> DEV u32 ld4(rsrc_t r, u32 off) { return __builtin_amdgcn_raw_buffer_load_b32(r, off, 0, AUX); }
template <int AUX> DEV void st16(rsrc_t r, u32 off, u32x4 v) { __builtin_amdgcn_raw_buffer_store_b128(v, r, off, 0, AUX); }
template <int AUX> DEV void st8(rsrc_t r, u32 off, u32x2 v) { __builtin_amdgcn_raw_buffer_store_b64(v, r, off, 0, AUX); }
template <int AUX> DEV void st4(rsrc_t r, u32 off, u32 v) { __builtin_amdgcn_raw_buffer_store_b32(v, r, off, 0, AUX); }
DEV u32x4 lds_ld16(const char* s, u32 off) { return *(const LAS u32x4*)((const LAS char*)s + off); }
DEV u32x2 lds_ld8(const char* s, u32 off) { return *(const LAS u32x2*)((const LAS char*)s + off); }
DEV u32 lds_ld4(const char* s, u32 off) { return *(const LAS u32*)((const LAS char*)s + off); }
DEV void lds_st16(char* s, u32 off, u32x4 v) { *(LAS u32x4*)((LAS char*)s + off) = v; }
DEV void lds_st8(char* s, u32 off, u32x2 v) { *(LAS u32x2*)((LAS char*)s + off) = v; }
DEV void lds_st4(char* s, u32 off, u32 v) { *(LAS u32*)((LAS char*)s + off) = v; }
DEV u32x2 lds_tr(const char* s, u32 off) {
  return __builtin_bit_cast(u32x2, __builtin_amdgcn_ds_read_tr16_b64_v4i16((LAS s16x4*)((LAS char*)s + off)));
}
DEV float f_(u32 x) { return __builtin_bit_cast(float, x); }
DEV u32 u_(float x) { return __builtin_bit_cast(u32, x); }
template <int CTRL> DEV float dpp_add(float v) {
  return v + __builtin_bit_cast(float, __builtin_amdgcn_update_dpp(0, __builtin_bit_cast(int, v), CTRL, 0xf, 0xf, true));
}
DEV float row_sum16(float v) { v = dpp_add<0xB1>(v); v = dpp_add<0x4E>(v); v = dpp_add<0x141>(v); return dpp_add<0x140>(v); }
DEV float lane_of(float v, int l) { return __builtin_bit_cast(float, __builtin_amdgcn_readlane(__builtin_bit_cast(int, v), l)); }
DEV float xor16(float v) { return __shfl_xor(v, 16); }
DEV float xor32(float v) { return __shfl_xor(v, 32); }
DEV void bar() { asm volatile("s_waitcnt lgkmcnt(0)\n\ts_barrier" ::: "memory"); }
DEV int otid() { int t = threadIdx.x; asm volatile("" : "+v"(t)); return t; }
DEV void sfence() { __builtin_amdgcn_sched_barrier(0); }
constexpr u32 SENT = 0xffffffffu;
constexpr int SC1 = 16;
constexpr int SPIN_CAP = 1 << 14;
DEV bool isbad(u32x4 v) { return max(max(v[0], v[1]), max(v[2], v[3])) == SENT; }
DEV bool isbad(u32x2 v) { return max(v[0], v[1]) == SENT; }
DEV bool anyb(bool b) { return __builtin_amdgcn_ballot_w64(b) != 0ull; }
template <int N, class F> DEV void repoll16(rsrc_t r, F off, u32x4 (&v)[N]) {
  for (int spin = 0; spin < SPIN_CAP; ++spin) {
    u32 mask = 0;
#pragma unroll
    for (int i = 0; i < N; ++i) mask |= anyb(isbad(v[i])) ? (1u << i) : 0u;
    if (!mask) break;
    __builtin_amdgcn_s_sleep(1);
#pragma unroll
    for (int i = 0; i < N; ++i) if (mask & (1u << i)) v[i] = ld16<SC1>(r, off(i));
  }
}
template <int N, int G, bool CHECK = true, class F> DEV void poll16(rsrc_t r, F off, u32x4 (&v)[N]) {
  constexpr int g = G >= 0 ? G : 0;
  if (G >= 0) {
    v[g] = ld16<SC1>(r, off(g));
    for (int spin = 0; spin < SPIN_CAP; ++spin) {
      if (!anyb(isbad(v[g]))) break;
      __builtin_amdgcn_s_sleep(1);
      v[g] = ld16<SC1>(r, off(g));
    }
  }
#pragma unroll
  for (int i = 0; i < N; ++i) if (i != G) v[i] = ld16<SC1>(r, off(i));
  if (CHECK) repoll16<N>(r, off, v);
}
DEV int permk(int g, int j) { return j < 4 ? 4 * g + j : 16 + 4 * g + (j - 4); }
DEV void ln_stats(float sum, float sq, float& m, float& rinv) {
  float mean = sum * (1.0f / 512.0f);
  float var = fmaxf(sq * (1.0f / 512.0f) - mean * mean, 0.0f);
  m = rbf(mean);
  float den = rbf(__builtin_amdgcn_sqrtf(rbf(rbf(var) + 1e-5f)));
  rinv = __builtin_amdgcn_rcpf(den);
}
DEV f32x2 unpk(u32 w) { return f32x2{bflo(w), bfhi(w)}; }
DEV f32x2 rbf2(f32x2 v) { return unpk(pk2(v[0], v[1])); }
DEV f32x2 lo2(f32x4 v) { return f32x2{v[0], v[1]}; }
DEV f32x2 hi2(f32x4 v) { return f32x2{v[2], v[3]}; }
DEV u32 phi2(f32x2 a) {
  f32x2 t = rbf2(a), s = t * 1.4426950408889634f, p = t + 1.0f;
  f32x2 e = {__builtin_amdgcn_exp2f(s[0]), __builtin_amdgcn_exp2f(s[1])};
  f32x2 q = {t[0] > 0.0f ? p[0] : e[0], t[1] > 0.0f ? p[1] : e[1]};
  q = rbf2(q) + 1e-6f;
  return pk2(q[0], q[1]);
}
constexpr u32 KA_ROW = 528;
constexpr u32 KA_TAB = 16 * KA_ROW;
constexpr u32 KA_G = 0, KA_E = 8 * KA_TAB;
constexpr u32 KA_KC = 16 * KA_TAB;
constexpr u32 KA_XT = KA_KC + 2 * 656 * 4;
constexpr u32 KA_LDS = KA_XT + 8 * 1040;
constexpr u32 KA_WIN = 0, KA_WSTR = 2064 * 16;
constexpr u32 KA_R = 100352;
typedef f32x2 cf;
DEV cf cmul(cf a, cf b) { return f32x2{a[0], a[0]} * b + f32x2{a[1], a[1]} * f32x2{-b[1], b[0]}; }
DEV cf cexp_pow(float are, float aim, int k) {
  float s = (float)(1 << k);
  float mag = __expf(are * s);
  double rev = (double)aim * (0.15915494309189535 * (double)(1 << k));
  float fr = (float)(rev - __builtin_floor(rev));
  return f32x2{__builtin_amdgcn_cosf(fr), __builtin_amdgcn_sinf(fr)} * mag;
}
DEV void phase_A(const Params& p, rsrc_t rw, char* sm, int d0, u32x4& xv0, u32x4& s0, u32x4& s1) {
  const int tid = otid(), b = blockIdx.x, lane = tid & 63, w = tid >> 6;
  const int n = tid & 127, ch = (tid >> 7) & 1, sih = tid >> 8;
  const int d = d0 + ch;
  const u32 rlA = p.logA[d * 128 + n], rAi = p.Aim[d * 128 + n], rCr = p.Cre[d * 128 + n], rCi = p.Cim[d * 128 + n], rdt = p.logdt[d];
  const u32 tag = ld4<SC1>(rw, O_EPO) + 1u;
  sfence();
  const int rg = (b & 7) * 32 + (b >> 3);
  const int row = rg * 8 + w;
  u32x4 xv = *(const u32x4*)(p.u + (size_t)row * 256 + lane * 4);
  u32 wv[8]; u32 wdst;
  {
    int g = b * NT + tid, ln = g & 63, lq = ln >> 4;
    const unsigned short* src; int ld, ks;
    if (g < 98304) {
      int f = g >> 6, ty = f % 3, f2 = f / 3, ks8 = f2 & 7, kh = (f2 >> 3) & 1, hh = (f2 >> 4) & 3, cb = f2 >> 6;
      ks = kh * 8 + ks8; src = p.wqkv + ty * 512 + (4 * cb + hh) * 16 + (ln & 15); ld = 1536; wdst = O_WQ + (u32)g * 16;
    } else {
      int g2 = g - 98304, f = g2 >> 6; ks = f & 15; int ww = (f >> 4) & 7, cb4 = f >> 7;
      src = p.wo + 128 * cb4 + 16 * ww + (ln & 15); ld = 512; wdst = O_WO + (u32)g2 * 16;
    }
#pragma unroll
    for (int j = 0; j < 8; ++j) wv[j] = src[(size_t)(32 * ks + permk(lq, j)) * ld];
  }
  {
    const u32 g = (u32)(b * NT + tid) * 16;
    const u32x4 sv = {SENT, SENT, SENT, SENT};
    st16<SC1>(rw, O_X1 + g, sv);
    if (g < O_FLG - O_ZP) st16<SC1>(rw, O_ZP + g, sv);
    if (g < 4 * L * 8) st16<SC1>(rw, O_ST2 + g, sv);
  }
  sfence();
  const float lA = bf2f(rlA), Ai = bf2f(rAi), Cr = bf2f(rCr), Ci = bf2f(rCi), dt = rbf(expf(bf2f(rdt)));
  float are = -rbf(expf(lA)) * dt, aim = Ai * dt;
  cf C = {Cr * dt, Ci * dt};
#pragma unroll
  for (int t = 0; t < 2; ++t) {
    const int si = sih + 2 * t;
    const u32 tb = (u32)(ch * 4 + si) * KA_TAB + n * 4;
    cf v0 = cexp_pow(are, aim, si), v1 = cmul(v0, v0), v2 = cmul(v1, v1), v3 = cmul(v2, v2);
    cf e[8]; e[0] = f32x2{1.f, 0.f}; e[1] = v0; e[2] = v1; e[3] = cmul(v1, v0); e[4] = v2;
    e[5] = cmul(v2, v0); e[6] = cmul(v2, v1); e[7] = cmul(v2, e[3]);
#pragma unroll
    for (int j = 0; j < 8; ++j) {
      lds_st4(sm, KA_E + tb + j * KA_ROW, pk2(e[j][0], e[j][1]));
      cf f = cmul(e[j], v3);
      lds_st4(sm, KA_E + tb + (j + 8) * KA_ROW, pk2(f[0], f[1]));
    }
    cf g0 = cexp_pow(are, aim, 4 + si), g1 = cmul(g0, g0), g2 = cmul(g1, g1), g3 = cmul(g2, g2);
    cf h[8]; h[0] = (si == 0) ? C : cmul(C, g3);
    h[1] = cmul(h[0], g0); h[2] = cmul(h[0], g1); h[3] = cmul(h[1], g1);
#pragma unroll
    for (int j = 0; j < 4; ++j) h[4 + j] = cmul(h[j], g2);
#pragma unroll
    for (int j = 0; j < 8; ++j) {
      lds_st4(sm, KA_G + tb + j * KA_ROW, pk2(h[j][0], -h[j][1]));
      cf f = cmul(h[j], g3);
      lds_st4(sm, KA_G + tb + (j + 8) * KA_ROW, pk2(f[0], -f[1]));
    }
  }
  sfence();
  {
    float s = 0.f, q = 0.f;
#pragma unroll
    for (int j = 0; j < 4; ++j) { s = dot2(xv[j], 0x3f803f80u, s); q = dot2(xv[j], xv[j], q); }
    s = row_sum16(s); q = row_sum16(q);
    s = (lane_of(s, 0) + lane_of(s, 16)) + (lane_of(s, 32) + lane_of(s, 48));
    q = (lane_of(q, 0) + lane_of(q, 16)) + (lane_of(q, 32) + lane_of(q, 48));
    if (lane == 0) { u32x2 v = {u_(s), u_(q)}; st8<SC1>(rw, O_ST0 + row * 8, v); }
    lds_st16(sm, KA_XT + w * 1040 + lane * 16, xv);
  }
  { u32x4 v = {wv[0] | (wv[1] << 16), wv[2] | (wv[3] << 16), wv[4] | (wv[5] << 16), wv[6] | (wv[7] << 16)}; st16<SC1>(rw, wdst, v); }
  bar();
  {
    u32 v[8];
#pragma unroll
    for (int r = 0; r < 8; ++r) v[r] = *(const LAS unsigned short*)((LAS char*)sm + KA_XT + r * 1040 + tid * 2);
    u32x4 o = {v[0] | (v[1] << 16), v[2] | (v[3] << 16), v[4] | (v[5] << 16), v[6] | (v[7] << 16)};
    st16<SC1>(rw, O_X0 + (((rg >> 2) * 512 + tid) * 32 + (rg & 3) * 8) * 2, o);
  }
  sfence();
  {
    const int wc = w >> 2, si = w & 3, lq = lane >> 4, r16 = lane & 15;
    const u32 base = (u32)(wc * 4 + si) * KA_TAB + r16 * KA_ROW + lq * 16;
    f32x4 acc = {0.f, 0.f, 0.f, 0.f};
#pragma unroll
    for (int ks = 0; ks < 8; ++ks) acc = mfma32(lds_ld16(sm, KA_G + base + ks * 64), lds_ld16(sm, KA_E + base + ks * 64), acc);
#pragma unroll
    for (int r = 0; r < 4; ++r) {
      int i = 4 * lq + r;
      int idx = si == 0 ? 16 * i + r16 : 256 + (si - 1) * 128 + 16 * i + r16;
      bool ok = si == 0 || i < 8 || (si == 3 && i == 8 && r16 == 0);
      if (ok) lds_st4(sm, KA_KC + (wc * 656 + idx) * 4, u_(2.0f * acc[r]));
    }
  }
  bar();
  const int c2 = tid >> 8, m = tid & 255;
  {
    u32 o[4];
#pragma unroll
    for (int j = 0; j < 4; ++j) {
      float kv[2];
#pragma unroll
      for (int h = 0; h < 2; ++h) {
        int l = 2047 - (8 * m + 2 * j + h);
        int e = l < 256 ? 0 : (l < 512 ? 1 : (l < 1024 ? 2 : 3));
        int mm = (l + ((1 << e) >> 1) - 1 + ((l >> e) & 1)) >> e;
        int idx = e == 0 ? l : 256 + (e - 1) * 128 + (mm - 128);
        kv[h] = f_(lds_ld4(sm, KA_KC + (c2 * 656 + idx) * 4));
      }
      o[j] = pk2(kv[0], kv[1]);
    }
    lds_st16(sm, KA_R + c2 * KR_STR + m * 16, u32x4{o[0], o[1], o[2], o[3]});
    if (m < 2) lds_st16(sm, KA_R + c2 * KR_STR + 4096 + m * 16, u32x4{0u, 0u, 0u, 0u});
  }
  asm volatile("s_waitcnt vmcnt(0)" ::: "memory");
  bar();
  if (tid == 0) st4<SC1>(rw, O_FLG + b * FLG_STR, tag);
#pragma unroll
  for (int i = 0; i < 4; ++i) {
    const u32 rb_ = KA_R + c2 * KR_STR + (m + 256 * i) * 4;
    u32 a[5];
#pragma unroll
    for (int j = 0; j < 5; ++j) a[j] = lds_ld4(sm, rb_ + j * 4);
    const u32 gb = KA_WIN + c2 * KA_WSTR + (m + 256 * i) * 32;
    lds_st16(sm, gb, u32x4{a[0], a[1], a[2], a[3]});
    lds_st16(sm, gb + 16, u32x4{__builtin_amdgcn_alignbit(a[1], a[0], 16), __builtin_amdgcn_alignbit(a[2], a[1], 16),
                                __builtin_amdgcn_alignbit(a[3], a[2], 16), __builtin_amdgcn_alignbit(a[4], a[3], 16)});
  }
  if (m < 16) lds_st16(sm, KA_WIN + c2 * KA_WSTR + (2048 + m) * 16, u32x4{0u, 0u, 0u, 0u});
  if (w < 4) {
    u32 fl = ld4<SC1>(rw, O_FLG + tid * FLG_STR);
    for (int spin = 0; spin < SPIN_CAP; ++spin) {
      if (!anyb(fl != tag)) break;
      __builtin_amdgcn_s_sleep(1);
      fl = ld4<SC1>(rw, O_FLG + tid * FLG_STR);
    }
  }
  bar();
  if (b == 0 && tid == 0) st4<SC1>(rw, O_EPO, tag);
  xv0 = ld16<SC1>(rw, O_X0 + (((m >> 2) * 512 + d0 + c2) * 32 + (m & 3) * 8) * 2);
  s0 = ld16<SC1>(rw, O_ST0 + tid * 32); s1 = ld16<SC1>(rw, O_ST0 + tid * 32 + 16);
}
constexpr u32 P_G = 0, P_GSTR = 2064 * 16;
constexpr u32 DB = 2 * P_GSTR;
constexpr u32 P_CS = 158720;
constexpr u32 P_LDS = P_CS + 8 * 3 * 16 * 4;
constexpr u32 C_U = DB, C_ST = C_U + 2 * 4608, C_RED = C_ST + L * 8;
constexpr u32 Q_A = DB, Q_EX = DB, Q_SP = DB + 65536, Q_MR = Q_SP + 4096;
constexpr u32 O_OF = DB, O_T = DB + 32768, O_PS = O_T + 128 * 144;
constexpr u32 R_B = DB;
static_assert(KA_LDS <= P_CS && KA_WIN == P_G && KA_WSTR == P_GSTR && KA_R + 2 * KR_STR <= KA_KC && KA_R >= C_RED && C_RED + 65536 <= P_CS && Q_MR + 8 * 512 <= P_CS && P_LDS <= 163840, "LDS map");


DEV void fill16(rsrc_t rw, u32 off) { st16<SC1>(rw, off, u32x4{SENT, SENT, SENT, SENT}); }
template <int NP, bool POLL>
DEV void phase_C(rsrc_t rw, char* sm, u32x4 xv0, u32x4 s0, u32x4 s1, u32x4 (&W)[8][3], int cb, int d0, float dsk, u32 o_in, u32 o_st, u32 o_out, int sb) {
  const int tid = otid(), lane = tid & 63, w = tid >> 6, ch = tid >> 8, m = tid & 255;
  const u32 xoff = (((m >> 2) * 512 + d0 + ch) * 32 + (m & 3) * 8) * 2;
  u32x4 xv;
  {
    u32x4 sv[2 * NP + 1];
    auto off = [&](int i) -> u32 { return i == 2 * NP ? o_in + xoff : o_st + (i >> 1) * L * 8 + tid * 32 + (i & 1) * 16; };
    if (POLL) poll16<2 * NP + 1, (POLL ? 2 * NP : -1)>(rw, off, sv);
    else {
      sv[0] = s0; sv[1] = s1; sv[2 * NP] = xv0;
      const u32 fg = (u32)(blockIdx.x * NT + tid) * 16;
      fill16(rw, O_PQ + fg); fill16(rw, O_KVP + fg);
    }
    xv = sv[2 * NP];
    float mr[8];
#pragma unroll
    for (int r = 0; r < 4; ++r) {
      float s = 0.f, q2 = 0.f;
#pragma unroll
      for (int q = 0; q < NP; ++q) { s += f_(sv[2 * q + (r >> 1)][(r & 1) * 2]); q2 += f_(sv[2 * q + (r >> 1)][(r & 1) * 2 + 1]); }
      ln_stats(s, q2, mr[2 * r], mr[2 * r + 1]);
    }
    lds_st16(sm, C_ST + tid * 32, u32x4{u_(mr[0]), u_(mr[2]), u_(mr[1]), u_(mr[3])});
    lds_st16(sm, C_ST + tid * 32 + 16, u32x4{u_(mr[4]), u_(mr[6]), u_(mr[5]), u_(mr[7])});
    if (tid < 64) lds_st16(sm, C_U + (tid >> 5) * 4608 + ((tid >> 4) & 1) * 2304 + (tid & 15) * 16, u32x4{0u, 0u, 0u, 0u});
  }
  asm volatile("s_waitcnt vmcnt(0)" ::: "memory");
  bar();
  u32x4 uv;
#pragma unroll
  for (int j = 0; j < 4; ++j) {
    u32x4 s = lds_ld16(sm, C_ST + (8 * m + 2 * j) * 8);
    f32x2 t = rbf2(unpk(xv[j]) - f32x2{f_(s[0]), f_(s[1])}) * f32x2{f_(s[2]), f_(s[3])};
    uv[j] = pk2(t[0], t[1]);
  }
  lds_st16(sm, C_U + ch * 4608 + (m & 1) * 2304 + (16 + (m >> 1)) * 16, uv);
  bar();
  {
    const int wc = w >> 2, w4 = w & 3, lq = lane >> 4, r16 = lane & 15;
    const int j0 = (lq & 1) * 16 + (lq >> 1) * 8, rho = 2 * w4;
    const u32 ub = C_U + wc * 4608 + (lq >> 1) * 2304 + (15 - 2 * rho + r16 + (lq & 1)) * 16;
    const u32 gb = P_G + wc * P_GSTR + (2031 - 32 * (rho + 1) - 256 * 7 - r16 + j0) * 16;
    f32x4 acc[8];
#pragma unroll
    for (int c = 0; c < 8; ++c) acc[c] = f32x4{0.f, 0.f, 0.f, 0.f};
    u32x4 B0[8], A0[8], B1[8], A1[8];
#pragma unroll
    for (int k = 0; k < 8; ++k) { if (k == 0) A0[0] = lds_ld16(sm, gb + 512 + 7 * 4096); B0[7 - k] = lds_ld16(sm, ub + (7 - k) * 256); }
#pragma unroll
    for (int k = 1; k < 8; ++k) A0[k] = lds_ld16(sm, gb + 512 + (7 - k) * 4096);
    sfence();
    const u32 wb = O_WQ + (u32)(cb * 8 + (w & 3) * 2 + (w >> 2)) * 24576 + lane * 16;
#pragma unroll
    for (int g = 0; g < 8; ++g) {
      A1[g] = lds_ld16(sm, gb + (7 - g) * 4096); B1[g] = lds_ld16(sm, ub - 32 + g * 256);
#pragma unroll
      for (int i = 2 * g; i < 2 * g + 2; ++i) W[i / 3][i % 3] = ld16<SC1>(rw, wb + i * 1024);
      sfence();
#pragma unroll
      for (int c = 7; c >= g; --c) acc[c] = mfma32(A0[g], B0[c - g], acc[c]);
      sfence();
    }
#pragma unroll
    for (int g = 0; g < 8; ++g) {
      W[(16 + g) / 3][(16 + g) % 3] = ld16<SC1>(rw, wb + (16 + g) * 1024);
      sfence();
#pragma unroll
      for (int c = g; c < 8; ++c) acc[c] = mfma32(A1[g], B1[c - g], acc[c]);
      sfence();
    }
#pragma unroll
    for (int c = 0; c < 8; ++c) lds_st16(sm, C_RED + ((wc * 4 + w4) * 8 + c) * 1024 + lane * 16, __builtin_bit_cast(u32x4, acc[c]));
  }
  bar();
  {
    const int c = m >> 5, a = (m & 31) >> 1, lq0 = (m & 1) * 2;
    f32x4 y0 = {0.f, 0.f, 0.f, 0.f}, y1 = y0;
#pragma unroll
    for (int q = 0; q < 4; ++q) {
      const u32 o = C_RED + ((ch * 4 + q) * 8 + c) * 1024 + (lq0 * 16 + a) * 16;
      y0 += __builtin_bit_cast(f32x4, lds_ld16(sm, o)); y1 += __builtin_bit_cast(f32x4, lds_ld16(sm, o + 256));
    }
    u32x4 ov;
#pragma unroll
    for (int j = 0; j < 4; ++j) {
      f32x2 y = j == 0 ? lo2(y0) : j == 1 ? hi2(y0) : j == 2 ? lo2(y1) : hi2(y1);
      f32x2 r = unpk(xv[j]) + rbf2(rbf2(y) + rbf2(unpk(uv[j]) * dsk));
      ov[j] = pk2(r[0], r[1]);
    }
    st16<SC1>(rw, o_out + xoff, ov);
  }
  bar();
}

DEV void q_colsum(char* sm, const u32x4 (&W)[8][3]) {
  const int tid = otid(), lane = tid & 63, w = tid >> 6;
#pragma unroll
  for (int ty = 0; ty < 3; ++ty) {
    float s = 0.f;
#pragma unroll
    for (int ks = 0; ks < 8; ++ks)
#pragma unroll
      for (int j = 0; j < 4; ++j) s = dot2(W[ks][ty][j], 0x3f803f80u, s);
    s += xor16(s); s += xor32(s);
    if (lane < 16) lds_st4(sm, P_CS + ((((w & 3) * 2 + (w >> 2)) * 3 + ty) * 16 + lane) * 4, u_(s));
  }
}
template <bool FIRST>
DEV void phase_Q(rsrc_t rw, char* sm, const u32x4 (&W)[8][3], u32x4 (&Wo)[16], int rb, int cb, int cb4, u32 o_x, u32 o_pq, u32 o_kvp, u32 o_zp,
                 int sb) {
  const int tid = otid(), lane = tid & 63, w = tid >> 6, hh = w & 3, kh = w >> 2, lq = lane >> 4, r16 = lane & 15;
  {
    u32x4 av[8];
    auto off = [&](int i) -> u32 { return o_x + (u32)rb * 65536 + (tid + i * 512) * 16; };
    poll16<8, 0, false>(rw, off, av);
    const int chn = tid >> 2, pc = tid & 3, f = (chn >> 1) & 3;
    auto stage = [&]() {
#pragma unroll
      for (int i = 0; i < 8; ++i)
        lds_st16(sm, Q_A + (chn + 128 * (i & 3)) * 128 + ((((i >> 2) * 2 + (pc >> 1)) ^ f) * 32) + (pc & 1) * 16, av[i]);
    };
    stage();
    bool bad = false;
#pragma unroll
    for (int i = 0; i < 8; ++i) bad = bad || isbad(av[i]);
    if (anyb(bad)) { repoll16<8>(rw, off, av); stage(); }
  }
  bar();
  f32x4 acc[4][3];
#pragma unroll
  for (int rt = 0; rt < 4; ++rt)
#pragma unroll
    for (int ty = 0; ty < 3; ++ty) acc[rt][ty] = f32x4{0.f, 0.f, 0.f, 0.f};
  const int chl = 4 * lq + (r16 >> 2), f = (chl >> 1) & 3;
  u32 ab[4];
#pragma unroll
  for (int rt = 0; rt < 4; ++rt) ab[rt] = Q_A + (kh * 256 + chl) * 128 + ((rt ^ f) * 32) + (r16 & 3) * 8;
  const u32 abs_ = Q_A + (kh * 256 + chl) * 128 + ((hh ^ f) * 32) + (r16 & 3) * 8;
  float ssum = 0.f, ssq = 0.f;
  const u32 wob = O_WO + (u32)(cb4 * 8 + w) * 16384 + lane * 16;
  {
    u32x2 xa[2][4][2], xs[2][2];
#pragma unroll
    for (int rt = 0; rt < 4; ++rt) { xa[0][rt][0] = lds_tr(sm, ab[rt]); xa[0][rt][1] = lds_tr(sm, ab[rt] + 2048); }
    xs[0][0] = lds_tr(sm, abs_); xs[0][1] = lds_tr(sm, abs_ + 2048);
#pragma unroll
    for (int ks = 0; ks < 8; ++ks) {
      const int cu = ks & 1, nx = cu ^ 1;
      if (ks < 7) {
#pragma unroll
        for (int rt = 0; rt < 4; ++rt) { xa[nx][rt][0] = lds_tr(sm, ab[rt] + (ks + 1) * 4096); xa[nx][rt][1] = lds_tr(sm, ab[rt] + (ks + 1) * 4096 + 2048); }
        xs[nx][0] = lds_tr(sm, abs_ + (ks + 1) * 4096); xs[nx][1] = lds_tr(sm, abs_ + (ks + 1) * 4096 + 2048);
      }
      Wo[2 * ks] = ld16<SC1>(rw, wob + 2 * ks * 1024); Wo[2 * ks + 1] = ld16<SC1>(rw, wob + (2 * ks + 1) * 1024);
      if (FIRST && ks == 1) fill16(rw, O_X2 + (u32)(blockIdx.x * NT + tid) * 16);
      if (FIRST && ks == 4) fill16(rw, O_X3 + (u32)(blockIdx.x * NT + tid) * 16);
      sfence();
#pragma unroll
      for (int rt = 0; rt < 4; ++rt) {
        u32x4 X = {xa[cu][rt][0][0], xa[cu][rt][0][1], xa[cu][rt][1][0], xa[cu][rt][1][1]};
        acc[rt][0] = mfma32(W[ks][0], X, acc[rt][0]);
        acc[rt][1] = mfma32(X, W[ks][1], acc[rt][1]);
        acc[rt][2] = mfma32(X, W[ks][2], acc[rt][2]);
      }
#pragma unroll
      for (int j = 0; j < 2; ++j)
#pragma unroll
        for (int e = 0; e < 2; ++e) { ssum = dot2(xs[cu][j][e], 0x3f803f80u, ssum); ssq = dot2(xs[cu][j][e], xs[cu][j][e], ssq); }
      sfence();
    }
  }
  lds_st8(sm, Q_SP + ((kh * 4 + lq) * 64 + hh * 16 + r16) * 8, u32x2{u_(ssum), u_(ssq)});
  bar();
  const u32 ex = Q_EX + hh * 12288 + lane * 16;
  if (kh == 0) {
#pragma unroll
    for (int rt = 0; rt < 4; ++rt) lds_st16(sm, ex + rt * 1024, __builtin_bit_cast(u32x4, acc[rt][0]));
  } else {
#pragma unroll
    for (int rt = 0; rt < 4; ++rt) {
      lds_st16(sm, ex + (4 + rt * 2) * 1024, __builtin_bit_cast(u32x4, acc[rt][1]));
      lds_st16(sm, ex + (5 + rt * 2) * 1024, __builtin_bit_cast(u32x4, acc[rt][2]));
    }
  }
  {
    float sum = 0.f, sq = 0.f;
#pragma unroll
    for (int i = 0; i < 8; ++i) { u32x2 pp = lds_ld8(sm, Q_SP + (i * 64 + lane) * 8); sum += f_(pp[0]); sq += f_(pp[1]); }
    float mean, rinv; ln_stats(sum, sq, mean, rinv);
    lds_st4(sm, Q_MR + w * 512 + lane * 4, u_(mean)); lds_st4(sm, Q_MR + w * 512 + 256 + lane * 4, u_(rinv));
  }
  bar();
  const int h = cb * 4 + hh;
  const u32 mrb = Q_MR + w * 512, csb = P_CS + hh * 2 * 3 * 16 * 4;
  if (kh == 1) {
    const f32x4 cs = __builtin_bit_cast(f32x4, lds_ld16(sm, csb + 16 * lq)) + __builtin_bit_cast(f32x4, lds_ld16(sm, csb + 192 + 16 * lq));
#pragma unroll
    for (int rp = 0; rp < 2; ++rp) {
      u32x4 ov;
#pragma unroll
      for (int t = 0; t < 2; ++t) {
        const int rt = rp * 2 + t;
        f32x4 a = acc[rt][0] + __builtin_bit_cast(f32x4, lds_ld16(sm, ex + rt * 1024));
        const float mean = f_(lds_ld4(sm, mrb + (rt * 16 + r16) * 4)), rinv = f_(lds_ld4(sm, mrb + 256 + (rt * 16 + r16) * 4));
        ov[2 * t] = phi2((lo2(a) - lo2(cs) * mean) * rinv); ov[2 * t + 1] = phi2((hi2(a) - hi2(cs) * mean) * rinv);
      }
      st16<SC1>(rw, o_pq + (((rb * 2 + rp) * 32 + h) * 64 + lane) * 16, ov);
    }
  } else {
    const float csk = f_(lds_ld4(sm, csb + 64 + r16 * 4)) + f_(lds_ld4(sm, csb + 192 + 64 + r16 * 4));
    const float csv = f_(lds_ld4(sm, csb + 128 + r16 * 4)) + f_(lds_ld4(sm, csb + 192 + 128 + r16 * 4));
    f32x4 kv = {0.f, 0.f, 0.f, 0.f}, zz = kv;
#pragma unroll
    for (int rp = 0; rp < 2; ++rp) {
      u32x4 pk, vv;
#pragma unroll
      for (int t = 0; t < 2; ++t) {
        const int rt = rp * 2 + t;
        f32x4 ak = acc[rt][1] + __builtin_bit_cast(f32x4, lds_ld16(sm, ex + (4 + rt * 2) * 1024));
        f32x4 av = acc[rt][2] + __builtin_bit_cast(f32x4, lds_ld16(sm, ex + (5 + rt * 2) * 1024));
        const f32x4 mean = __builtin_bit_cast(f32x4, lds_ld16(sm, mrb + (rt * 16 + 4 * lq) * 4));
        const f32x4 rinv = __builtin_bit_cast(f32x4, lds_ld16(sm, mrb + 256 + (rt * 16 + 4 * lq) * 4));
        pk[2 * t] = phi2((lo2(ak) - lo2(mean) * csk) * lo2(rinv)); pk[2 * t + 1] = phi2((hi2(ak) - hi2(mean) * csk) * hi2(rinv));
        f32x2 v0 = (lo2(av) - lo2(mean) * csv) * lo2(rinv), v1 = (hi2(av) - hi2(mean) * csv) * hi2(rinv);
        vv[2 * t] = pk2(v0[0], v0[1]); vv[2 * t + 1] = pk2(v1[0], v1[1]);
      }
      kv = mfma32(pk, vv, kv);
      zz = mfma32(pk, u32x4{0x3f803f80u, 0x3f803f80u, 0x3f803f80u, 0x3f803f80u}, zz);
    }
    st16<SC1>(rw, o_kvp + ((rb * 32 + h) * 64 + lane) * 16, __builtin_bit_cast(u32x4, kv));
    if (r16 == 0) st16<SC1>(rw, o_zp + (rb * 32 + h) * 64 + lq * 16, __builtin_bit_cast(u32x4, zz));
  }
  bar();
}

DEV void phase_R(rsrc_t rw, char* sm, int b, u32 o_kvp, u32 o_zp, u32 o_kvt, u32 o_zt) {
  const int tid = otid(), h = b >> 3, j = b & 7, rb = tid >> 4, q = tid & 15;
  u32x4 v[1];
  const u32 off = q < 8 ? o_kvp + ((rb * 32 + h) * 64 + 8 * j + q) * 16 : o_zp + (rb * 32 + h) * 64 + (j >> 1) * 16;
  poll16<1, -1>(rw, [&](int) -> u32 { return off; }, v);
  asm volatile("s_waitcnt vmcnt(0)" ::: "memory");
  if (q < 8) lds_st16(sm, R_B + (rb * 40 + q * 4) * 4, v[0]);
  else if (q == 8) lds_st8(sm, R_B + (rb * 40 + 32) * 4, (j & 1) ? u32x2{v[0][2], v[0][3]} : u32x2{v[0][0], v[0][1]});
  bar();
  if (tid < 64) {
    const int e = tid < 34 ? tid : 0;
    float s = 0.f;
#pragma unroll
    for (int r = 0; r < 32; ++r) s += f_(lds_ld4(sm, R_B + (r * 40 + e) * 4));
    float o = __shfl_down(s, 1), o2 = __shfl_down(s, 2), o3 = __shfl_down(s, 3);
    if (tid < 32 && (tid & 3) == 0) st8<SC1>(rw, o_kvt + (h * 64 + 8 * j + (tid >> 2)) * 8, u32x2{pk2(s, o), pk2(o2, o3)});
    if (tid == 32) st4<SC1>(rw, o_zt + h * 32 + j * 4, pk2(s, o));
  }
  bar();
}

DEV void o_prefetch(rsrc_t rw, u32x4 (&v)[5], int rb, int cb4, u32 o_pq, u32 o_xin) {
  const int tid = otid(), lane = tid & 63, w = tid >> 6;
#pragma unroll
  for (int i = 0; i < 4; ++i) v[i] = ld16<SC1>(rw, o_pq + ((rb * 32 + 4 * w + i) * 64 + lane) * 16);
  v[4] = ld16<SC1>(rw, o_xin + ((rb * 512 + cb4 * 128 + (tid >> 2)) * 32 + (tid & 3) * 8) * 2);
}
template <bool FINAL>
DEV void phase_O(unsigned short* out, rsrc_t rw, char* sm, const u32x4 (&Wo)[16], u32x4 (&v)[5], int rb, int cb4, u32 o_pq, u32 o_kvt, u32 o_zt,
                 u32 o_xin, u32 o_xout, u32 o_st, int sb) {
  const int tid = otid(), lane = tid & 63, w = tid >> 6, lq = lane >> 4, r16 = lane & 15;
  const int ecol = tid >> 2, epc = tid & 3;
  const u32 xoff = ((rb * 512 + cb4 * 128 + ecol) * 32 + epc * 8) * 2;
  u32x4 xin;
  {
    u32x2 kv[4], zt[4];
    kv[3] = ld8<SC1>(rw, o_kvt + ((4 * w + 3) * 64 + lane) * 8);
    for (int spin = 0; spin < SPIN_CAP; ++spin) {
      if (!anyb(isbad(kv[3]))) break;
      __builtin_amdgcn_s_sleep(1);
      kv[3] = ld8<SC1>(rw, o_kvt + ((4 * w + 3) * 64 + lane) * 8);
    }
#pragma unroll
    for (int i = 0; i < 4; ++i) { if (i < 3) kv[i] = ld8<SC1>(rw, o_kvt + ((4 * w + i) * 64 + lane) * 8); zt[i] = ld8<SC1>(rw, o_zt + (4 * w + i) * 32 + lq * 8); }
    repoll16<5>(rw, [&](int i) -> u32 { return i < 4 ? o_pq + ((rb * 32 + 4 * w + i) * 64 + lane) * 16 : o_xin + xoff; }, v);
    xin = v[4];
    for (int spin = 0; spin < SPIN_CAP; ++spin) {
      bool bad = false;
#pragma unroll
      for (int i = 0; i < 4; ++i) bad = bad || isbad(kv[i]) || isbad(zt[i]);
      if (!anyb(bad)) break;
      __builtin_amdgcn_s_sleep(1);
#pragma unroll
      for (int i = 0; i < 4; ++i) { kv[i] = ld8<SC1>(rw, o_kvt + ((4 * w + i) * 64 + lane) * 8); zt[i] = ld8<SC1>(rw, o_zt + (4 * w + i) * 32 + lq * 8); }
    }
#pragma unroll
    for (int hp = 0; hp < 2; ++hp)
#pragma unroll
      for (int rt = 0; rt < 2; ++rt) {
        u32x4 of;
#pragma unroll
        for (int t = 0; t < 2; ++t) {
          const int i = hp * 2 + t;
          u32x2 B = {v[i][2 * rt], v[i][2 * rt + 1]};
          f32x4 z4 = {0.f, 0.f, 0.f, 0.f};
          f32x4 num = mfma16(kv[i], B, z4), den = mfma16(zt[i], B, z4);
          const float dr = __builtin_amdgcn_rcpf(rbf(rbf(den[0]) + 1e-6f));
          f32x2 o0 = rbf2(lo2(num)) * dr, o1 = rbf2(hi2(num)) * dr;
          of[2 * t] = pk2(o0[0], o0[1]); of[2 * t + 1] = pk2(o1[0], o1[1]);
        }
        lds_st16(sm, O_OF + ((rt * 16 + 2 * w + hp) * 64 + lane) * 16, of);
      }
  }
  bar();
  {
    f32x4 a0 = {0.f, 0.f, 0.f, 0.f}, a1 = a0;
    u32x4 o0[16], o1[16];
#pragma unroll
    for (int ks = 0; ks < 4; ++ks) { o0[ks] = lds_ld16(sm, O_OF + (ks * 64 + lane) * 16); o1[ks] = lds_ld16(sm, O_OF + ((16 + ks) * 64 + lane) * 16); }
#pragma unroll
    for (int ks = 0; ks < 16; ++ks) {
      if (!FINAL && ks == 2) fill16(rw, O_PQ + XSZ + (u32)(blockIdx.x * NT + tid) * 16);
      if (ks + 4 < 16) { o0[ks + 4] = lds_ld16(sm, O_OF + ((ks + 4) * 64 + lane) * 16); o1[ks + 4] = lds_ld16(sm, O_OF + ((20 + ks) * 64 + lane) * 16); }
      sfence();
      a0 = mfma32(o0[ks], Wo[ks], a0);
      a1 = mfma32(o1[ks], Wo[ks], a1);
      sfence();
    }
    lds_st16(sm, O_T + (16 * w + r16) * 144 + (4 * lq) * 4, __builtin_bit_cast(u32x4, a0));
    lds_st16(sm, O_T + (16 * w + r16) * 144 + (16 + 4 * lq) * 4, __builtin_bit_cast(u32x4, a1));
  }
  bar();
  f32x4 t0 = __builtin_bit_cast(f32x4, lds_ld16(sm, O_T + ecol * 144 + epc * 32)), t1 = __builtin_bit_cast(f32x4, lds_ld16(sm, O_T + ecol * 144 + epc * 32 + 16));
  u32x4 ov;
#pragma unroll
  for (int j = 0; j < 4; ++j) {
    f32x2 r = unpk(xin[j]) + rbf2(j == 0 ? lo2(t0) : j == 1 ? hi2(t0) : j == 2 ? lo2(t1) : hi2(t1));
    ov[j] = pk2(r[0], r[1]);
  }
  if (!FINAL) st16<SC1>(rw, o_xout + xoff, ov);
  lds_st16(sm, O_OF + ecol * 80 + epc * 16, ov);
  bar();
  const int rt = w & 1, cq = w >> 1;
  const u32 tb = O_OF + (cq * 32 + 8 * lq + (r16 >> 2)) * 80 + rt * 32 + (r16 & 3) * 8;
  const u32x2 c0 = lds_tr(sm, tb), c1 = lds_tr(sm, tb + 4 * 80);
  if (!FINAL) {
    float s = dot2(c0[0], 0x3f803f80u, 0.f), q = dot2(c0[0], c0[0], 0.f);
    s = dot2(c0[1], 0x3f803f80u, s); q = dot2(c0[1], c0[1], q);
    s = dot2(c1[0], 0x3f803f80u, s); q = dot2(c1[0], c1[0], q);
    s = dot2(c1[1], 0x3f803f80u, s); q = dot2(c1[1], c1[1], q);
    lds_st8(sm, O_PS + ((cq * 4 + lq) * 32 + rt * 16 + r16) * 8, u32x2{u_(s), u_(q)});
    bar();
    if (tid < 32) {
      float sum = 0.f, sq = 0.f;
#pragma unroll
      for (int i = 0; i < 16; ++i) { u32x2 pp = lds_ld8(sm, O_PS + (i * 32 + tid) * 8); sum += f_(pp[0]); sq += f_(pp[1]); }
      st8<SC1>(rw, o_st + (cb4 * L + rb * 32 + tid) * 8, u32x2{u_(sum), u_(sq)});
    }
    bar();
  } else {
    *(u32x4*)(out + (size_t)(rb * 32 + rt * 16 + r16) * 512 + cb4 * 128 + cq * 32 + lq * 8) = u32x4{c0[0], c0[1], c1[0], c1[1]};
  }
}

extern __shared__ __attribute__((aligned(16))) char smem[];
__global__ void __launch_bounds__(512, 2) kB(Params p) {
  rsrc_t rw = mk_rsrc(p.ws, O_END);
  const int b = blockIdx.x, tid = otid();
  const int d0 = 2 * ((b & 7) * 32 + (b >> 3));
  const int rbq = (b & 7) * 4 + ((b >> 3) & 3), cb = b >> 5;
  const int rbo = (b & 7) * 8 + ((b >> 3) & 7), cb4 = b >> 6;
  const float dsk = bf2f(p.Dskip[d0 + (tid >> 8)]);
  u32 pcv;
  {
    unsigned long long pc; u32 len;
    asm volatile("s_getpc_b64 %0\nkb_text_begin:\n\ts_mov_b32 %1, kb_text_end-kb_text_begin" : "=s"(pc), "=s"(len));
    const u32 o = ((u32)(b >> 3) * NT + tid) * 4;
    pcv = *(const __attribute__((address_space(1))) u32*)(pc + min(o, len - 4u));
  }
  u32x4 W[8][3], Wo[16], ov5[5], xv0, s0, s1;
  phase_A(p, rw, smem, d0, xv0, s0, s1);
  asm volatile("" :: "v"(pcv));
  phase_C<1, false>(rw, smem, xv0, s0, s1, W, cb, d0, dsk, O_X0, O_ST0, O_X1, 3);
  q_colsum(smem, W);
  phase_Q<true>(rw, smem, W, Wo, rbq, cb, cb4, O_X1, O_PQ, O_KVP, O_ZP, 6);
  phase_R(rw, smem, b, O_KVP, O_ZP, O_KVT, O_ZT);
  o_prefetch(rw, ov5, rbo, cb4, O_PQ, O_X1);
  phase_O<false>(p.out, rw, smem, Wo, ov5, rbo, cb4, O_PQ, O_KVT, O_ZT, O_X1, O_X2, O_ST2, 10);
  phase_C<4, true>(rw, smem, xv0, s0, s1, W, cb, d0, dsk, O_X2, O_ST2, O_X3, 13);
  phase_Q<false>(rw, smem, W, Wo, rbq, cb, cb4, O_X3, O_PQ + XSZ, O_KVP + KVP_SZ, O_ZP + ZP_SZ, 16);
  phase_R(rw, smem, b, O_KVP + KVP_SZ, O_ZP + ZP_SZ, O_KVT + KVT_SZ, O_ZT + ZT_SZ);
  o_prefetch(rw, ov5, rbo, cb4, O_PQ + XSZ, O_X3);
  phase_O<true>(p.out, rw, smem, Wo, ov5, rbo, cb4, O_PQ + XSZ, O_KVT + KVT_SZ, O_ZT + ZT_SZ, O_X3, 0u, 0u, 20);
  asm volatile("kb_text_end:");
}
static Params make_params(void* const* d_in, void* d_out, void* d_ws) {
  Params p;
  p.u = (const u32*)d_in[0]; p.logA = (const unsigned short*)d_in[1]; p.Aim = (const unsigned short*)d_in[2];
  p.Cre = (const unsigned short*)d_in[3]; p.Cim = (const unsigned short*)d_in[4]; p.logdt = (const unsigned short*)d_in[5];
  p.Dskip = (const unsigned short*)d_in[6]; p.wqkv = (const unsigned short*)d_in[8]; p.wo = (const unsigned short*)d_in[9];
  p.ws = (char*)d_ws; p.out = (unsigned short*)d_out;
  return p;
}
extern "C" void kernel_launch(void* const* d_in, const int* in_sizes, int n_in, void* d_out, int out_size, void* d_ws, size_t ws_size,
                              hipStream_t stream) {
  Params p = make_params(d_in, d_out, d_ws);
  (void)hipFuncSetAttribute((const void*)kB, hipFuncAttributeMaxDynamicSharedMemorySize, P_LDS);
  kB<<<NB, NT, P_LDS, stream>>>(p);
}
```
